```python
import torch

def _softmax(x, axis=-1):
    return torch.softmax(x, dim=axis)
def _gelu(x):
    return torch.nn.functional.gelu(x, approximate="tanh")
def _silu(x):
    return torch.nn.functional.silu(x)
def _sigmoid(x):
    return torch.sigmoid(x)
def _layernorm(x, axis=-1, eps=1e-5):
    m = x.mean(dim=axis, keepdim=True)
    v = x.var(dim=axis, keepdim=True, unbiased=False)
    return (x - m) / torch.sqrt(v + eps)
def _scatter_add(tgt, idx, src, axis=0):
    return tgt.clone().index_add_(axis, idx, src)
def _segment_sum(src, seg, n):
    out = torch.zeros((n,) + src.shape[1:], dtype=src.dtype)
    return out.index_add_(0, seg, src)
def _pad(x, pw):
    flat = tuple(v for pair in reversed(pw) for v in pair)
    return torch.nn.functional.pad(x, flat)

kernel_name = 'recurrent_scan__spectral_gfnet__swiglu_ffn__prompt'

def setup_inputs():

    _w = (lambda a: (lambda t: t.to(torch.bfloat16) if t.is_floating_point() else t)(torch.as_tensor(a)))
    def _A_setup():
        B, S, D = 1, 2048, 448
        Df = D // 2 + 1
        return {
            "x": _w((lambda *s: torch.randn(*s, dtype=torch.bfloat16) * 0.05)(B, S, D)),
            "w_r": _w((lambda *s: torch.randn(*s, dtype=torch.bfloat16) * 0.2)(S, Df) * 0.02),
            "w_i": _w((lambda *s: torch.randn(*s, dtype=torch.bfloat16) * 0.2)(S, Df) * 0.02),
        }
    _A_in = _A_setup()

    def _B_setup():
        B, S, D, F = 1, 2048, 448, 1792
        return {
            "x": _w((lambda *s: torch.randn(*s, dtype=torch.bfloat16) * 0.05)(B, S, D)),
            "w_gate": _w((lambda *s: torch.randn(*s, dtype=torch.bfloat16) * 0.2)(D, F)),
            "w_up": _w((lambda *s: torch.randn(*s, dtype=torch.bfloat16) * 0.2)(D, F)),
            "w_down": _w((lambda *s: torch.randn(*s, dtype=torch.bfloat16) * 0.2)(F, D)),
        }
    _B_in = _B_setup()

    return {
        "A_x": _A_in["x"],
        "A_w_r": _A_in["w_r"],
        "A_w_i": _A_in["w_i"],
        "B_x": _B_in["x"],
        "B_w_gate": _B_in["w_gate"],
        "B_w_up": _B_in["w_up"],
        "B_w_down": _B_in["w_down"],
    }

def reference(A_x, A_w_r, A_w_i, B_x, B_w_gate, B_w_up, B_w_down):
    def _A_ref(x, w_r, w_i):
        B, S, D = x.shape
        X = (lambda x, axes=(-2,-1): torch.fft.rfft2(x.float(), dim=axes))(x, axes=(-2, -1))
        W = w_r + 1j * w_i
        X = X * W
        y = (lambda x, s, axes=(-2,-1): torch.fft.irfft2(x, s=s, dim=axes).to(torch.bfloat16))(X, (S, D), axes=(-2, -1))
        return y

    def _B_ref(x, w_gate, w_up, w_down):
        g = _silu(x @ w_gate)
        u = x @ w_up
        return (g * u) @ w_down

    x = A_x
    xn = _layernorm(x, axis=-1)
    s = _A_ref(xn, A_w_r, A_w_i)
    g = _silu(_B_ref(xn, B_w_gate, B_w_up, B_w_down))
    return x + s * g
```

```cpp
#include <hip/hip_runtime.h>
#include <hip/hip_bf16.h>
#include <stdint.h>
typedef unsigned short u16;
constexpr int S=2048, D=448, DF=225, F=1792;
__device__ inline float bf2f(u16 v){return __uint_as_float(((unsigned)v)<<16);}
__device__ inline u16 f2bf(float x){unsigned u=__float_as_uint(x);u+=0x7fffu+((u>>16)&1u);return (u16)(u>>16);}
__device__ inline float rbf(float x){return bf2f(f2bf(x));}

__global__ void k_ln(const u16* x, float* xn){
  int row=blockIdx.x; int lane=threadIdx.x;
  float v[7]; float s=0;
  for(int i=0;i<7;i++){v[i]=bf2f(x[row*D+i*64+lane]); s+=v[i];}
  for(int o=32;o>0;o>>=1) s+=__shfl_xor(s,o);
  float m=s/D; float q=0;
  for(int i=0;i<7;i++){float d=v[i]-m;q+=d*d;}
  for(int o=32;o>0;o>>=1) q+=__shfl_xor(q,o);
  float var=rbf(q/D); float mb=rbf(m);
  float den=rbf(sqrtf(rbf(var+1e-5f)));
  for(int i=0;i<7;i++) xn[row*D+i*64+lane]=rbf(rbf(v[i]-mb)/den);
}
__global__ void k_rowdft(const float* xn, float2* X1){
  __shared__ float c[D], sn[D], xr[D];
  int s=blockIdx.x;
  for(int i=threadIdx.x;i<D;i+=256){c[i]=cospif(2.f*i/D); sn[i]=sinpif(2.f*i/D); xr[i]=xn[s*D+i];}
  __syncthreads();
  int k=threadIdx.x; if(k>=DF) return;
  float re=0,im=0; int p=0;
  for(int d=0;d<D;d++){re+=xr[d]*c[p]; im-=xr[d]*sn[p]; p+=k; if(p>=D)p-=D;}
  X1[k*S+s]=make_float2(re,im);
}
__global__ void k_coldft(const float2* in, float2* out, const u16* wr, const u16* wi, int fwd){
  __shared__ float2 t[S]; __shared__ float2 xs[S];
  int k=blockIdx.x;
  for(int i=threadIdx.x;i<S;i+=256){t[i]=make_float2(cospif(2.f*i/S), sinpif(2.f*i/S)*(fwd?-1.f:1.f)); xs[i]=in[k*S+i];}
  __syncthreads();
  int m=blockIdx.y*256+threadIdx.x;
  float re=0,im=0; int p=0;
  for(int s=0;s<S;s++){float2 a=xs[s], w=t[p]; re+=a.x*w.x-a.y*w.y; im+=a.x*w.y+a.y*w.x; p=(p+m)&(S-1);}
  if(fwd){float a=bf2f(wr[m*DF+k]), b=bf2f(wi[m*DF+k]); float r2=re*a-im*b, i2=re*b+im*a; re=r2; im=i2;}
  out[k*S+m]=make_float2(re,im);
}
__global__ void k_gemm1(const float* xn,const u16* wg,const u16* wu,float* h){
  __shared__ float xr[8][D];
  int r0=blockIdx.y*8;
  for(int i=threadIdx.x;i<8*D;i+=256) xr[i/D][i%D]=xn[r0*D+i];
  __syncthreads();
  int f=blockIdx.x*256+threadIdx.x;
  float g[8]={0},u[8]={0};
  for(int d=0;d<D;d++){float a=bf2f(wg[d*F+f]), b=bf2f(wu[d*F+f]);
    for(int r=0;r<8;r++){g[r]+=xr[r][d]*a;u[r]+=xr[r][d]*b;}}
  for(int r=0;r<8;r++){float gg=rbf(g[r]); float sg=rbf(gg/(1.f+__expf(-gg))); h[(r0+r)*F+f]=rbf(sg*rbf(u[r]));}
}
__global__ void k_final(const u16* x,const float* h,const u16* wd,const float2* Z,u16* out){
  __shared__ float hr[4][F]; __shared__ float2 z[4][DF]; __shared__ float c[D], sn[D];
  int r0=blockIdx.x*4;
  for(int i=threadIdx.x;i<4*F;i+=448) hr[i/F][i%F]=h[r0*F+i];
  for(int i=threadIdx.x;i<4*DF;i+=448) z[i%4][i/4]=Z[(i/4)*S+r0+(i%4)];
  {int i=threadIdx.x; c[i]=cospif(2.f*i/D); sn[i]=sinpif(2.f*i/D);}
  __syncthreads();
  int d=threadIdx.x;
  float g[4]={0},y[4]={0};
  for(int f=0;f<F;f++){float w=bf2f(wd[f*D+d]); for(int r=0;r<4;r++) g[r]+=hr[r][f]*w;}
  int p=0;
  for(int k=0;k<DF;k++){float sc=(k==0||k==DF-1)?1.f:2.f; float cc=c[p]*sc, ss=(k==0||k==DF-1)?0.f:sn[p]*sc;
    for(int r=0;r<4;r++) y[r]+=z[r][k].x*cc-z[r][k].y*ss; p+=d; if(p>=D)p-=D;}
  for(int r=0;r<4;r++){float s=rbf(y[r]*(1.f/(S*D))); float gg=rbf(g[r]); float sg=rbf(gg/(1.f+__expf(-gg)));
    out[(r0+r)*D+d]=f2bf(bf2f(x[(r0+r)*D+d])+rbf(s*sg));}
}
__global__ void k_empty(int* p){ if(p==nullptr) *p=0; }
#define NEXTRA 0
extern "C" void kernel_launch(void* const* d_in, const int* in_sizes, int n_in, void* d_out, int out_size, void* d_ws, size_t ws_size, hipStream_t stream){
  const u16* x=(const u16*)d_in[0]; const u16* wr=(const u16*)d_in[1]; const u16* wi=(const u16*)d_in[2];
  const u16* wg=(const u16*)d_in[4]; const u16* wu=(const u16*)d_in[5]; const u16* wd=(const u16*)d_in[6];
  char* ws=(char*)d_ws;
  float* xn=(float*)ws; float2* A=(float2*)(ws+(4<<20)); float2* B=(float2*)(ws+(8<<20)); float* h=(float*)(ws+(16<<20));
  k_ln<<<S,64,0,stream>>>(x,xn);
  k_rowdft<<<S,256,0,stream>>>(xn,A);
  k_coldft<<<dim3(DF,S/256),256,0,stream>>>(A,B,wr,wi,1);
  k_coldft<<<dim3(DF,S/256),256,0,stream>>>(B,A,wr,wi,0);
  k_gemm1<<<dim3(F/256,S/8),256,0,stream>>>(xn,wg,wu,h);
  k_final<<<S/4,448,0,stream>>>(x,h,wd,A,(u16*)d_out);
  for(int i=0;i<NEXTRA;i++) k_empty<<<256,256,0,stream>>>((int*)d_ws);
}
```

```cpp
#include <hip/hip_runtime.h>
#include <stdint.h>


typedef unsigned short u16;

__global__ __launch_bounds__(256, 1) void fused_kernel(const char* x, const u16* wr, const u16* wi, const char* wg,
                                                       const char* wu, const char* wd, char* out, char* ws) {
  __shared__ __attribute__((aligned(16))) char smem[163840];
  const int b = blockIdx.x, xcd = b & 7, idx = b >> 3;
  const int w = __builtin_amdgcn_readfirstlane(threadIdx.x >> 6);
  const auto ka = __builtin_amdgcn_kernarg_segment_ptr();
  const int wsel = 24 + 8 * (w >> 1);
  if (idx < 4) {
    asm volatile(
    ".Lp_begin:\n"
    "s_load_dwordx2 s[36:37], %0, 0\n"
    "s_load_dwordx2 s[52:53], %0, 56\n"
    "s_load_dwordx2 s[40:41], %0, %1\n"
    "s_load_dwordx2 s[44:45], %0, 8\n"
    "s_load_dwordx2 s[48:49], %0, 16\n"
    "v_mbcnt_lo_u32_b32 v0, -1, 0\n"
    "v_mbcnt_hi_u32_b32 v0, -1, v0\n"
    "s_mov_b32 s60, %5\n"
    "s_mov_b32 s61, %6\n"
    "s_getpc_b64 s[56:57]\n"
    ".Lp_pc:\n"
    "s_sub_u32 s56, s56, .Lp_pc-.Lm_begin\n"
    "s_subb_u32 s57, s57, 0\n"
    "s_and_b32 s57, s57, 0xffff\n"
    "s_mov_b32 s58, .Lp_end-.Lm_begin\n"
    "s_mov_b32 s59, 0x20000\n"
    "s_lshr_b32 s62, %7, 3\n"
    "s_xor_b32 s62, s62, 4\n"
    "s_lshl_b32 s62, s62, 2\n"
    "s_add_u32 s62, s62, s60\n"
    "s_lshl_b32 s62, s62, 10\n"
    "v_lshlrev_b32 v11, 4, v0\n"
    "buffer_load_dwordx4 v[252:255], v11, s[56:59], s62 offen offset:0\n"
    "s_lshr_b32 s96, s61, 1\n"
    "s_and_b32 s97, s61, 1\n"
    "s_add_u32 s97, s97, 14\n"
    "s_lshl_b32 s63, s97, 2\n"
    "s_add_u32 s63, s63, s60\n"
    "s_lshl_b32 s63, s63, 1\n"
    "s_bfe_u32 s62, s63, 0x50000\n"
    "s_lshl_b32 s62, s62, 4\n"
    "s_add_u32 s62, s62, s96\n"
    "s_lshl_b32 s62, s62, 2\n"
    "s_lshr_b32 s63, s63, 5\n"
    "s_add_u32 s62, s62, s63\n"
    "s_mul_i32 s62, s62, 896\n"
    "s_add_u32 s63, s62, 57344\n"
    "v_cmp_gt_u32 vcc, 56, v0\n"
    "v_lshlrev_b32 v3, 4, v0\n"
    "v_mov_b32 v20, 0x7ffffff0\n"
    "v_cndmask_b32 v3, v20, v3, vcc\n"
    "s_waitcnt lgkmcnt(0)\n"
    "s_and_b32 s37, s37, 0xffff\n"
    "s_mov_b32 s38, 0x1c0000\n"
    "s_mov_b32 s39, 0x20000\n"
    "buffer_load_dwordx4 v[20:23], v3, s[36:39], s62 offen offset:0\n"
    "buffer_load_dwordx4 v[24:27], v3, s[36:39], s63 offen offset:0\n"
    "s_and_b32 s53, s53, 0xffff\n"
    "s_mov_b32 s54, 0x1600000\n"
    "s_mov_b32 s55, 0x20000\n"
    "v_mov_b32 v9, 0\n"
    "buffer_load_dwordx2 v[248:249], v9, s[52:55], 0 offen offset:0 sc1\n"
    "s_and_b32 s41, s41, 0xffff\n"
    "s_mov_b32 s42, 0x188000\n"
    "s_mov_b32 s43, 0x20000\n"
    "s_cmp_lt_u32 s61, 28\n"
    "s_cselect_b32 s42, s42, 0\n"
    "s_and_b32 s62, s60, 1\n"
    "s_mul_i32 s62, s62, 802816\n"
    "s_lshl_b32 s63, s61, 7\n"
    "s_add_u32 s62, s62, s63\n"
    "v_lshrrev_b32 v9, 3, v0\n"
    "v_mul_u32_u24 v9, 3584, v9\n"
    "v_and_b32 v10, 7, v0\n"
    "v_lshl_add_u32 v9, v10, 4, v9\n"
    "v_add_u32 v5, s62, v9\n"
    "v_and_b32 v1, 31, v0\n"
    "v_lshrrev_b32 v2, 5, v0\n"
    "s_and_b32 s45, s45, 0xffff\n"
    "s_mov_b32 s46, 0xe1000\n"
    "s_mov_b32 s47, 0x20000\n"
    "s_and_b32 s49, s49, 0xffff\n"
    "s_mov_b32 s50, 0xe1000\n"
    "s_mov_b32 s51, 0x20000\n"
    "s_lshl_b32 s62, s60, 4\n"
    "v_add_u32 v9, s62, v2\n"
    "v_lshl_add_u32 v9, v9, 5, s61\n"
    "v_mul_u32_u24 v9, 450, v9\n"
    "v_and_b32 v9, -16, v9\n"
    "v_lshl_add_u32 v7, v1, 4, v9\n"
    "s_lshl_b32 s98, s60, 13\n"
    "s_add_u32 s98, s98, 65536\n"
    "v_lshl_add_u32 v9, v0, 5, s61\n"
    "v_sub_u32 v10, 2048, v9\n"
    "v_and_b32 v10, 2047, v10\n"
    "v_mul_u32_u24 v10, 450, v10\n"
    "s_lshr_b32 s62, s60, 1\n"
    "s_mul_i32 s62, s62, 448\n"
    "v_add_u32 v8, s62, v10\n"
    "s_bitcmp1_b32 s60, 0\n"
    "s_cselect_b64 s[88:89], s[48:49], s[44:45]\n"
    "s_mov_b64 s[90:91], s[46:47]\n"
    "s_load_dwordx2 s[56:57], %0, 40\n"
    "s_waitcnt lgkmcnt(0)\n"
    "s_and_b32 s57, s57, 0xffff\n"
    "s_mov_b32 s58, 0x188000\n"
    "s_mov_b32 s59, 0x20000\n"
    "s_mul_i32 s62, s61, 50176\n"
    "s_lshl_b32 s63, s60, 10\n"
    "s_add_u32 s62, s62, s63\n"
    "v_lshl_add_u32 v214, v0, 4, s62\n"
    "s_lshl_b32 s64, s60, 13\n"
    "s_add_u32 s64, s64, 32768\n"
    "s_mov_b32 s65, 0x0\n"
    "buffer_load_dwordx4 v[84:87], v5, s[40:43], s65 offen offset:0\n"
    "s_mov_b32 s65, 0x7000\n"
    "buffer_load_dwordx4 v[88:91], v5, s[40:43], s65 offen offset:0\n"
    "s_mov_b32 s65, 0xe000\n"
    "buffer_load_dwordx4 v[92:95], v5, s[40:43], s65 offen offset:0\n"
    "s_mov_b32 s65, 0x15000\n"
    "buffer_load_dwordx4 v[96:99], v5, s[40:43], s65 offen offset:0\n"
    "s_mov_b32 s65, 0x1c000\n"
    "buffer_load_dwordx4 v[100:103], v5, s[40:43], s65 offen offset:0\n"
    "s_mov_b32 s65, 0x23000\n"
    "buffer_load_dwordx4 v[104:107], v5, s[40:43], s65 offen offset:0\n"
    "v_mov_b32 v28, 0\n"
    "v_mov_b32 v29, 0\n"
    "v_mov_b32 v30, 0\n"
    "v_mov_b32 v31, 0\n"
    "s_mov_b32 s72, 0x3f803f80\n"
    "s_lshl_b32 s62, s96, 4\n"
    "s_add_u32 s62, s62, s97\n"
    "s_lshl_b32 s62, s62, 2\n"
    "s_add_u32 s62, s62, s60\n"
    "s_lshl_b32 s62, s62, 5\n"
    "s_add_u32 s62, s62, 135168\n"
    "v_lshrrev_b32 v3, 1, v0\n"
    "v_add_u32 v3, s62, v3\n"
    "v_and_b32 v32, 31, v0\n"
    "v_cmp_eq_u32 vcc, 0, v32\n"
    "v_mov_b32 v32, 0x7ffffff0\n"
    "s_waitcnt vmcnt(7)\n"
    "s_mov_b32 s65, 0x2a000\n"
    "buffer_load_dwordx4 v[108:111], v5, s[40:43], s65 offen offset:0\n"
    "s_nop 0\n"
    "v_cndmask_b32 v3, v32, v3, vcc\n"
    "v_dot2c_f32_bf16 v28, s72, v20\n"
    "v_dot2c_f32_bf16 v30, v20, v20\n"
    "v_dot2c_f32_bf16 v29, s72, v24\n"
    "v_dot2c_f32_bf16 v31, v24, v24\n"
    "v_dot2c_f32_bf16 v28, s72, v21\n"
    "v_dot2c_f32_bf16 v30, v21, v21\n"
    "v_dot2c_f32_bf16 v29, s72, v25\n"
    "v_dot2c_f32_bf16 v31, v25, v25\n"
    "v_dot2c_f32_bf16 v28, s72, v22\n"
    "v_dot2c_f32_bf16 v30, v22, v22\n"
    "s_mov_b32 s65, 0x31000\n"
    "buffer_load_dwordx4 v[112:115], v5, s[40:43], s65 offen offset:0\n"
    "v_dot2c_f32_bf16 v29, s72, v26\n"
    "v_dot2c_f32_bf16 v31, v26, v26\n"
    "v_dot2c_f32_bf16 v28, s72, v23\n"
    "v_dot2c_f32_bf16 v30, v23, v23\n"
    "v_dot2c_f32_bf16 v29, s72, v27\n"
    "v_dot2c_f32_bf16 v31, v27, v27\n"
    "s_waitcnt vmcnt(8)\n"
    "v_readfirstlane_b32 s74, v248\n"
    "v_readfirstlane_b32 s75, v249\n"
    "v_permlane32_swap_b32 v28, v29\n"
    "v_permlane32_swap_b32 v30, v31\n"
    "v_add_f32 v28, v28, v29\n"
    "v_add_f32 v30, v30, v31\n"
    "s_mul_i32 s74, s74, 1664525\n"
    "s_mul_i32 s75, s75, 22695477\n"
    "s_nop 1\n"
    "v_permlane16_swap_b32 v28, v30\n"
    "v_add_f32 v28, v28, v30\n"
    "s_add_u32 s74, s74, 1013904223\n"
    "s_add_u32 s75, s75, 0x9e3779b9\n"
    "s_nop 1\n"
    "v_add_f32_dpp v28, v28, v28 row_ror:8 row_mask:0xf bank_mask:0xf\n"
    "v_mov_b32 v248, s74\n"
    "v_mov_b32 v249, s75\n"
    "v_add_f32_dpp v28, v28, v28 row_ror:4 row_mask:0xf bank_mask:0xf\n"
    "v_mov_b32 v34, s74\n"
    "v_mov_b32 v35, s75\n"
    "v_add_f32_dpp v28, v28, v28 row_ror:2 row_mask:0xf bank_mask:0xf\n"
    "s_nop 1\n"
    "v_add_f32_dpp v28, v28, v28 row_ror:1 row_mask:0xf bank_mask:0xf\n"
    "v_mov_b32 v30, v28\n"
    "s_mov_b32 s65, 0x38000\n"
    "buffer_load_dwordx4 v[116:119], v5, s[40:43], s65 offen offset:0\n"
    "s_nop 1\n"
    "v_permlane16_swap_b32 v28, v30\n"
    "v_mul_f32 v28, 0x3b124925, v28\n"
    "v_mul_f32 v30, 0x3b124925, v30\n"
    "v_fma_f32 v30, -v28, v28, v30\n"
    "v_max_f32 v30, 0, v30\n"
    "v_cvt_pk_bf16_f32 v30, v30, v30\n"
    "v_lshlrev_b32 v30, 16, v30\n"
    "v_add_f32 v30, 0x3727c5ac, v30\n"
    "v_cvt_pk_bf16_f32 v30, v30, v30\n"
    "v_lshlrev_b32 v30, 16, v30\n"
    "v_sqrt_f32 v30, v30\n"
    "s_nop 0\n"
    "v_cvt_pk_bf16_f32 v32, v28, v30\n"
    "v_and_b32 v30, 0xffff0000, v32\n"
    "v_rcp_f32 v33, v30\n"
    "buffer_store_dwordx4 v[32:35], v3, s[52:55], 0 offen offset:0 sc1\n"
    "s_mov_b32 s70, 0x00003f80\n"
    "s_mov_b32 s71, 0x3f800000\n"
    "s_mov_b32 s65, 0x3f000\n"
    "buffer_load_dwordx4 v[120:123], v5, s[40:43], s65 offen offset:0\n"
    "v_and_b32 v9, 19, v1\n"
    "v_bfe_u32 v10, v1, 2, 1\n"
    "v_lshl_add_u32 v9, v10, 3, v9\n"
    "v_bfe_u32 v10, v1, 3, 1\n"
    "v_lshlrev_b32 v10, 2, v10\n"
    "s_lshr_b32 s62, s60, 1\n"
    "s_lshl_b32 s62, s62, 5\n"
    "v_add3_u32 v9, v9, v10, s62\n"
    "v_cvt_f32_u32 v11, v9\n"
    "v_mul_f32 v11, 0x3c800000, v11\n"
    "s_and_b32 s62, s60, 1\n"
    "s_lshl_b32 s62, s62, 5\n"
    "v_lshl_add_u32 v9, v2, 3, s62\n"
    "v_cvt_f32_u32 v9, v9\n"
    "v_mul_f32 v12, v11, v9\n"
    "s_lshl_b32 s62, s60, 11\n"
    "s_add_u32 s62, s62, 4096\n"
    "v_lshl_add_u32 v6, v0, 4, s62\n"
    "v_fmamk_f32 v13, v11, 0x00000000, v12\n"
    "v_fmamk_f32 v14, v11, 0x3f800000, v12\n"
    "v_cos_f32 v15, v13\n"
    "v_cos_f32 v16, v14\n"
    "v_sin_f32 v13, v13\n"
    "v_sin_f32 v14, v14\n"
    "s_mov_b32 s65, 0x46000\n"
    "buffer_load_dwordx4 v[124:127], v5, s[40:43], s65 offen offset:0\n"
    "v_cvt_pk_bf16_f32 v236, v15, v16\n"
    "v_cvt_pk_bf16_f32 v240, v13, v14\n"
    "v_cvt_pk_bf16_f32 v244, -v13, -v14\n"
    "v_fmamk_f32 v13, v11, 0x40000000, v12\n"
    "v_fmamk_f32 v14, v11, 0x40400000, v12\n"
    "v_cos_f32 v15, v13\n"
    "v_cos_f32 v16, v14\n"
    "v_sin_f32 v13, v13\n"
    "v_sin_f32 v14, v14\n"
    "v_cvt_pk_bf16_f32 v237, v15, v16\n"
    "v_cvt_pk_bf16_f32 v241, v13, v14\n"
    "v_cvt_pk_bf16_f32 v245, -v13, -v14\n"
    "v_fmamk_f32 v13, v11, 0x40800000, v12\n"
    "v_fmamk_f32 v14, v11, 0x40a00000, v12\n"
    "v_cos_f32 v15, v13\n"
    "v_cos_f32 v16, v14\n"
    "v_sin_f32 v13, v13\n"
    "s_mov_b32 s65, 0x4d000\n"
    "buffer_load_dwordx4 v[128:131], v5, s[40:43], s65 offen offset:0\n"
    "v_sin_f32 v14, v14\n"
    "v_cvt_pk_bf16_f32 v238, v15, v16\n"
    "v_cvt_pk_bf16_f32 v242, v13, v14\n"
    "v_cvt_pk_bf16_f32 v246, -v13, -v14\n"
    "v_fmamk_f32 v13, v11, 0x40c00000, v12\n"
    "v_fmamk_f32 v14, v11, 0x40e00000, v12\n"
    "v_cos_f32 v15, v13\n"
    "v_cos_f32 v16, v14\n"
    "v_sin_f32 v13, v13\n"
    "v_sin_f32 v14, v14\n"
    "v_cvt_pk_bf16_f32 v239, v15, v16\n"
    "v_cvt_pk_bf16_f32 v243, v13, v14\n"
    "v_cvt_pk_bf16_f32 v247, -v13, -v14\n"
    "ds_write_b128 v6, v[236:239] offset:0\n"
    "s_mov_b32 s65, 0x54000\n"
    "buffer_load_dwordx4 v[132:135], v5, s[40:43], s65 offen offset:0\n"
    "ds_write_b128 v6, v[240:243] offset:8192\n"
    "ds_write_b128 v6, v[244:247] offset:16384\n"
    "v_fmamk_f32 v13, v11, 0x41800000, v12\n"
    "s_mov_b32 s65, 0x5b000\n"
    "buffer_load_dwordx4 v[136:139], v5, s[40:43], s65 offen offset:0\n"
    "v_fmamk_f32 v14, v11, 0x41880000, v12\n"
    "v_cos_f32 v15, v13\n"
    "v_cos_f32 v16, v14\n"
    "v_sin_f32 v13, v13\n"
    "v_sin_f32 v14, v14\n"
    "v_cvt_pk_bf16_f32 v236, v15, v16\n"
    "v_cvt_pk_bf16_f32 v240, v13, v14\n"
    "v_cvt_pk_bf16_f32 v244, -v13, -v14\n"
    "v_fmamk_f32 v13, v11, 0x41900000, v12\n"
    "v_fmamk_f32 v14, v11, 0x41980000, v12\n"
    "v_cos_f32 v15, v13\n"
    "v_cos_f32 v16, v14\n"
    "v_sin_f32 v13, v13\n"
    "v_sin_f32 v14, v14\n"
    "v_cvt_pk_bf16_f32 v237, v15, v16\n"
    "v_cvt_pk_bf16_f32 v241, v13, v14\n"
    "s_mov_b32 s65, 0x62000\n"
    "buffer_load_dwordx4 v[140:143], v5, s[40:43], s65 offen offset:0\n"
    "v_cvt_pk_bf16_f32 v245, -v13, -v14\n"
    "v_fmamk_f32 v13, v11, 0x41a00000, v12\n"
    "v_fmamk_f32 v14, v11, 0x41a80000, v12\n"
    "v_cos_f32 v15, v13\n"
    "v_cos_f32 v16, v14\n"
    "v_sin_f32 v13, v13\n"
    "v_sin_f32 v14, v14\n"
    "v_cvt_pk_bf16_f32 v238, v15, v16\n"
    "v_cvt_pk_bf16_f32 v242, v13, v14\n"
    "v_cvt_pk_bf16_f32 v246, -v13, -v14\n"
    "v_fmamk_f32 v13, v11, 0x41b00000, v12\n"
    "v_fmamk_f32 v14, v11, 0x41b80000, v12\n"
    "v_cos_f32 v15, v13\n"
    "v_cos_f32 v16, v14\n"
    "v_sin_f32 v13, v13\n"
    "v_sin_f32 v14, v14\n"
    "s_mov_b32 s65, 0x69000\n"
    "buffer_load_dwordx4 v[144:147], v5, s[40:43], s65 offen offset:0\n"
    "v_cvt_pk_bf16_f32 v239, v15, v16\n"
    "v_cvt_pk_bf16_f32 v243, v13, v14\n"
    "v_cvt_pk_bf16_f32 v247, -v13, -v14\n"
    "ds_write_b128 v6, v[236:239] offset:1024\n"
    "ds_write_b128 v6, v[240:243] offset:9216\n"
    "s_mov_b32 s65, 0x70000\n"
    "buffer_load_dwordx4 v[180:183], v5, s[40:43], s65 offen offset:0\n"
    "ds_write_b128 v6, v[244:247] offset:17408\n"
    "v_mov_b32 v36, 0\n"
    "v_mov_b32 v37, 0\n"
    "v_mov_b32 v38, 0\n"
    "v_mov_b32 v39, 0\n"
    "v_mov_b32 v40, 0\n"
    "v_mov_b32 v41, 0\n"
    "v_mov_b32 v42, 0\n"
    "v_mov_b32 v43, 0\n"
    "s_waitcnt vmcnt(17)\n"
    "v_dot2c_f32_bf16 v36, s70, v84\n"
    "v_dot2c_f32_bf16 v37, s71, v84\n"
    "v_dot2c_f32_bf16 v38, s70, v85\n"
    "v_dot2c_f32_bf16 v39, s71, v85\n"
    "v_dot2c_f32_bf16 v40, s70, v86\n"
    "v_dot2c_f32_bf16 v41, s71, v86\n"
    "v_dot2c_f32_bf16 v42, s70, v87\n"
    "v_dot2c_f32_bf16 v43, s71, v87\n"
    "s_waitcnt vmcnt(16)\n"
    "v_dot2c_f32_bf16 v36, s70, v88\n"
    "v_dot2c_f32_bf16 v37, s71, v88\n"
    "v_dot2c_f32_bf16 v38, s70, v89\n"
    "v_dot2c_f32_bf16 v39, s71, v89\n"
    "v_dot2c_f32_bf16 v40, s70, v90\n"
    "v_dot2c_f32_bf16 v41, s71, v90\n"
    "v_dot2c_f32_bf16 v42, s70, v91\n"
    "v_dot2c_f32_bf16 v43, s71, v91\n"
    "s_waitcnt vmcnt(15)\n"
    "v_dot2c_f32_bf16 v36, s70, v92\n"
    "v_dot2c_f32_bf16 v37, s71, v92\n"
    "v_dot2c_f32_bf16 v38, s70, v93\n"
    "v_dot2c_f32_bf16 v39, s71, v93\n"
    "v_dot2c_f32_bf16 v40, s70, v94\n"
    "v_dot2c_f32_bf16 v41, s71, v94\n"
    "v_dot2c_f32_bf16 v42, s70, v95\n"
    "v_dot2c_f32_bf16 v43, s71, v95\n"
    "s_waitcnt vmcnt(14)\n"
    "v_dot2c_f32_bf16 v36, s70, v96\n"
    "v_dot2c_f32_bf16 v37, s71, v96\n"
    "v_dot2c_f32_bf16 v38, s70, v97\n"
    "v_dot2c_f32_bf16 v39, s71, v97\n"
    "v_dot2c_f32_bf16 v40, s70, v98\n"
    "v_dot2c_f32_bf16 v41, s71, v98\n"
    "v_dot2c_f32_bf16 v42, s70, v99\n"
    "v_dot2c_f32_bf16 v43, s71, v99\n"
    "s_waitcnt vmcnt(13)\n"
    "v_dot2c_f32_bf16 v36, s70, v100\n"
    "v_dot2c_f32_bf16 v37, s71, v100\n"
    "v_dot2c_f32_bf16 v38, s70, v101\n"
    "v_dot2c_f32_bf16 v39, s71, v101\n"
    "v_dot2c_f32_bf16 v40, s70, v102\n"
    "v_dot2c_f32_bf16 v41, s71, v102\n"
    "v_dot2c_f32_bf16 v42, s70, v103\n"
    "v_dot2c_f32_bf16 v43, s71, v103\n"
    "s_waitcnt vmcnt(12)\n"
    "v_dot2c_f32_bf16 v36, s70, v104\n"
    "v_dot2c_f32_bf16 v37, s71, v104\n"
    "v_dot2c_f32_bf16 v38, s70, v105\n"
    "v_dot2c_f32_bf16 v39, s71, v105\n"
    "v_dot2c_f32_bf16 v40, s70, v106\n"
    "v_dot2c_f32_bf16 v41, s71, v106\n"
    "v_dot2c_f32_bf16 v42, s70, v107\n"
    "v_dot2c_f32_bf16 v43, s71, v107\n"
    "s_waitcnt vmcnt(11)\n"
    "v_dot2c_f32_bf16 v36, s70, v108\n"
    "v_dot2c_f32_bf16 v37, s71, v108\n"
    "v_dot2c_f32_bf16 v38, s70, v109\n"
    "v_dot2c_f32_bf16 v39, s71, v109\n"
    "v_dot2c_f32_bf16 v40, s70, v110\n"
    "v_dot2c_f32_bf16 v41, s71, v110\n"
    "v_dot2c_f32_bf16 v42, s70, v111\n"
    "v_dot2c_f32_bf16 v43, s71, v111\n"
    "s_waitcnt vmcnt(10)\n"
    "v_dot2c_f32_bf16 v36, s70, v112\n"
    "v_dot2c_f32_bf16 v37, s71, v112\n"
    "v_dot2c_f32_bf16 v38, s70, v113\n"
    "v_dot2c_f32_bf16 v39, s71, v113\n"
    "v_dot2c_f32_bf16 v40, s70, v114\n"
    "v_dot2c_f32_bf16 v41, s71, v114\n"
    "v_dot2c_f32_bf16 v42, s70, v115\n"
    "v_dot2c_f32_bf16 v43, s71, v115\n"
    "s_waitcnt vmcnt(9)\n"
    "v_dot2c_f32_bf16 v36, s70, v116\n"
    "v_dot2c_f32_bf16 v37, s71, v116\n"
    "v_dot2c_f32_bf16 v38, s70, v117\n"
    "v_dot2c_f32_bf16 v39, s71, v117\n"
    "v_dot2c_f32_bf16 v40, s70, v118\n"
    "v_dot2c_f32_bf16 v41, s71, v118\n"
    "v_dot2c_f32_bf16 v42, s70, v119\n"
    "v_dot2c_f32_bf16 v43, s71, v119\n"
    "s_mov_b32 s65, 0x77000\n"
    "buffer_load_dwordx4 v[184:187], v5, s[40:43], s65 offen offset:0\n"
    "s_waitcnt vmcnt(8)\n"
    "v_dot2c_f32_bf16 v36, s70, v120\n"
    "v_dot2c_f32_bf16 v37, s71, v120\n"
    "v_dot2c_f32_bf16 v38, s70, v121\n"
    "v_dot2c_f32_bf16 v39, s71, v121\n"
    "v_dot2c_f32_bf16 v40, s70, v122\n"
    "v_dot2c_f32_bf16 v41, s71, v122\n"
    "v_dot2c_f32_bf16 v42, s70, v123\n"
    "v_dot2c_f32_bf16 v43, s71, v123\n"
    "s_waitcnt vmcnt(7)\n"
    "s_mov_b32 s65, 0x7e000\n"
    "buffer_load_dwordx4 v[188:191], v5, s[40:43], s65 offen offset:0\n"
    "v_dot2c_f32_bf16 v36, s70, v124\n"
    "v_dot2c_f32_bf16 v37, s71, v124\n"
    "v_dot2c_f32_bf16 v38, s70, v125\n"
    "v_dot2c_f32_bf16 v39, s71, v125\n"
    "v_dot2c_f32_bf16 v40, s70, v126\n"
    "v_dot2c_f32_bf16 v41, s71, v126\n"
    "v_dot2c_f32_bf16 v42, s70, v127\n"
    "v_dot2c_f32_bf16 v43, s71, v127\n"
    "s_waitcnt vmcnt(7)\n"
    "s_mov_b32 s65, 0x85000\n"
    "buffer_load_dwordx4 v[192:195], v5, s[40:43], s65 offen offset:0\n"
    "v_dot2c_f32_bf16 v36, s70, v128\n"
    "v_dot2c_f32_bf16 v37, s71, v128\n"
    "v_dot2c_f32_bf16 v38, s70, v129\n"
    "v_dot2c_f32_bf16 v39, s71, v129\n"
    "v_dot2c_f32_bf16 v40, s70, v130\n"
    "v_dot2c_f32_bf16 v41, s71, v130\n"
    "v_dot2c_f32_bf16 v42, s70, v131\n"
    "v_dot2c_f32_bf16 v43, s71, v131\n"
    "s_waitcnt vmcnt(7)\n"
    "s_mov_b32 s65, 0x8c000\n"
    "buffer_load_dwordx4 v[196:199], v5, s[40:43], s65 offen offset:0\n"
    "v_dot2c_f32_bf16 v36, s70, v132\n"
    "v_dot2c_f32_bf16 v37, s71, v132\n"
    "v_dot2c_f32_bf16 v38, s70, v133\n"
    "v_dot2c_f32_bf16 v39, s71, v133\n"
    "v_dot2c_f32_bf16 v40, s70, v134\n"
    "v_dot2c_f32_bf16 v41, s71, v134\n"
    "v_dot2c_f32_bf16 v42, s70, v135\n"
    "v_dot2c_f32_bf16 v43, s71, v135\n"
    "s_waitcnt vmcnt(7)\n"
    "s_mov_b32 s65, 0x93000\n"
    "buffer_load_dwordx4 v[200:203], v5, s[40:43], s65 offen offset:0\n"
    "v_dot2c_f32_bf16 v36, s70, v136\n"
    "v_dot2c_f32_bf16 v37, s71, v136\n"
    "v_dot2c_f32_bf16 v38, s70, v137\n"
    "v_dot2c_f32_bf16 v39, s71, v137\n"
    "v_dot2c_f32_bf16 v40, s70, v138\n"
    "v_dot2c_f32_bf16 v41, s71, v138\n"
    "v_dot2c_f32_bf16 v42, s70, v139\n"
    "v_dot2c_f32_bf16 v43, s71, v139\n"
    "s_waitcnt vmcnt(7)\n"
    "s_mov_b32 s65, 0x9a000\n"
    "buffer_load_dwordx4 v[204:207], v5, s[40:43], s65 offen offset:0\n"
    "v_dot2c_f32_bf16 v36, s70, v140\n"
    "v_dot2c_f32_bf16 v37, s71, v140\n"
    "v_dot2c_f32_bf16 v38, s70, v141\n"
    "v_dot2c_f32_bf16 v39, s71, v141\n"
    "v_dot2c_f32_bf16 v40, s70, v142\n"
    "v_dot2c_f32_bf16 v41, s71, v142\n"
    "v_dot2c_f32_bf16 v42, s70, v143\n"
    "v_dot2c_f32_bf16 v43, s71, v143\n"
    "s_waitcnt vmcnt(7)\n"
    "s_mov_b32 s65, 0xa1000\n"
    "buffer_load_dwordx4 v[208:211], v5, s[40:43], s65 offen offset:0\n"
    "v_dot2c_f32_bf16 v36, s70, v144\n"
    "v_dot2c_f32_bf16 v37, s71, v144\n"
    "v_dot2c_f32_bf16 v38, s70, v145\n"
    "v_dot2c_f32_bf16 v39, s71, v145\n"
    "v_dot2c_f32_bf16 v40, s70, v146\n"
    "v_dot2c_f32_bf16 v41, s71, v146\n"
    "v_dot2c_f32_bf16 v42, s70, v147\n"
    "v_dot2c_f32_bf16 v43, s71, v147\n"
    "s_waitcnt vmcnt(7)\n"
    "s_mov_b32 s65, 0xa8000\n"
    "buffer_load_dwordx4 v[216:219], v5, s[40:43], s65 offen offset:0\n"
    "v_dot2c_f32_bf16 v36, s70, v180\n"
    "v_dot2c_f32_bf16 v37, s71, v180\n"
    "v_dot2c_f32_bf16 v38, s70, v181\n"
    "v_dot2c_f32_bf16 v39, s71, v181\n"
    "v_dot2c_f32_bf16 v40, s70, v182\n"
    "v_dot2c_f32_bf16 v41, s71, v182\n"
    "v_dot2c_f32_bf16 v42, s70, v183\n"
    "v_dot2c_f32_bf16 v43, s71, v183\n"
    "s_waitcnt vmcnt(7)\n"
    "s_mov_b32 s65, 0xaf000\n"
    "buffer_load_dwordx4 v[220:223], v5, s[40:43], s65 offen offset:0\n"
    "v_dot2c_f32_bf16 v36, s70, v184\n"
    "v_dot2c_f32_bf16 v37, s71, v184\n"
    "v_dot2c_f32_bf16 v38, s70, v185\n"
    "v_dot2c_f32_bf16 v39, s71, v185\n"
    "v_dot2c_f32_bf16 v40, s70, v186\n"
    "v_dot2c_f32_bf16 v41, s71, v186\n"
    "v_dot2c_f32_bf16 v42, s70, v187\n"
    "v_dot2c_f32_bf16 v43, s71, v187\n"
    "s_waitcnt vmcnt(7)\n"
    "s_mov_b32 s65, 0xb6000\n"
    "buffer_load_dwordx4 v[224:227], v5, s[40:43], s65 offen offset:0\n"
    "v_dot2c_f32_bf16 v36, s70, v188\n"
    "v_dot2c_f32_bf16 v37, s71, v188\n"
    "v_dot2c_f32_bf16 v38, s70, v189\n"
    "v_dot2c_f32_bf16 v39, s71, v189\n"
    "v_dot2c_f32_bf16 v40, s70, v190\n"
    "v_dot2c_f32_bf16 v41, s71, v190\n"
    "v_dot2c_f32_bf16 v42, s70, v191\n"
    "v_dot2c_f32_bf16 v43, s71, v191\n"
    "s_waitcnt vmcnt(7)\n"
    "s_mov_b32 s65, 0xbd000\n"
    "buffer_load_dwordx4 v[228:231], v5, s[40:43], s65 offen offset:0\n"
    "v_dot2c_f32_bf16 v36, s70, v192\n"
    "v_dot2c_f32_bf16 v37, s71, v192\n"
    "v_dot2c_f32_bf16 v38, s70, v193\n"
    "v_dot2c_f32_bf16 v39, s71, v193\n"
    "v_dot2c_f32_bf16 v40, s70, v194\n"
    "v_dot2c_f32_bf16 v41, s71, v194\n"
    "v_dot2c_f32_bf16 v42, s70, v195\n"
    "v_dot2c_f32_bf16 v43, s71, v195\n"
    "s_waitcnt vmcnt(7)\n"
    "v_dot2c_f32_bf16 v36, s70, v196\n"
    "v_dot2c_f32_bf16 v37, s71, v196\n"
    "v_dot2c_f32_bf16 v38, s70, v197\n"
    "v_dot2c_f32_bf16 v39, s71, v197\n"
    "v_dot2c_f32_bf16 v40, s70, v198\n"
    "v_dot2c_f32_bf16 v41, s71, v198\n"
    "v_dot2c_f32_bf16 v42, s70, v199\n"
    "v_dot2c_f32_bf16 v43, s71, v199\n"
    "s_waitcnt vmcnt(6)\n"
    "buffer_load_ushort v167, v8, s[88:91], 0 offen offset:0\n"
    "v_dot2c_f32_bf16 v36, s70, v200\n"
    "v_dot2c_f32_bf16 v37, s71, v200\n"
    "v_dot2c_f32_bf16 v38, s70, v201\n"
    "v_dot2c_f32_bf16 v39, s71, v201\n"
    "v_dot2c_f32_bf16 v40, s70, v202\n"
    "v_dot2c_f32_bf16 v41, s71, v202\n"
    "v_dot2c_f32_bf16 v42, s70, v203\n"
    "v_dot2c_f32_bf16 v43, s71, v203\n"
    "s_waitcnt vmcnt(6)\n"
    "s_add_u32 m0, s98, 0x0\n"
    "s_mov_b32 s73, 0x0\n"
    "s_nop 0\n"
    "buffer_load_dwordx4 v7, s[44:47], s73 offen offset:0 lds\n"
    "v_dot2c_f32_bf16 v36, s70, v204\n"
    "v_dot2c_f32_bf16 v37, s71, v204\n"
    "v_dot2c_f32_bf16 v38, s70, v205\n"
    "v_dot2c_f32_bf16 v39, s71, v205\n"
    "v_dot2c_f32_bf16 v40, s70, v206\n"
    "v_dot2c_f32_bf16 v41, s71, v206\n"
    "v_dot2c_f32_bf16 v42, s70, v207\n"
    "v_dot2c_f32_bf16 v43, s71, v207\n"
    "s_waitcnt vmcnt(6)\n"
    "s_add_u32 m0, s98, 0x400\n"
    "s_mov_b32 s73, 0x7080\n"
    "s_nop 0\n"
    "buffer_load_dwordx4 v7, s[44:47], s73 offen offset:0 lds\n"
    "v_dot2c_f32_bf16 v36, s70, v208\n"
    "v_dot2c_f32_bf16 v37, s71, v208\n"
    "v_dot2c_f32_bf16 v38, s70, v209\n"
    "v_dot2c_f32_bf16 v39, s71, v209\n"
    "v_dot2c_f32_bf16 v40, s70, v210\n"
    "v_dot2c_f32_bf16 v41, s71, v210\n"
    "v_dot2c_f32_bf16 v42, s70, v211\n"
    "v_dot2c_f32_bf16 v43, s71, v211\n"
    "s_waitcnt vmcnt(6)\n"
    "s_add_u32 m0, s98, 0x800\n"
    "s_mov_b32 s73, 0xe100\n"
    "s_nop 0\n"
    "buffer_load_dwordx4 v7, s[44:47], s73 offen offset:0 lds\n"
    "v_dot2c_f32_bf16 v36, s70, v216\n"
    "v_dot2c_f32_bf16 v37, s71, v216\n"
    "v_dot2c_f32_bf16 v38, s70, v217\n"
    "v_dot2c_f32_bf16 v39, s71, v217\n"
    "v_dot2c_f32_bf16 v40, s70, v218\n"
    "v_dot2c_f32_bf16 v41, s71, v218\n"
    "v_dot2c_f32_bf16 v42, s70, v219\n"
    "v_dot2c_f32_bf16 v43, s71, v219\n"
    "s_waitcnt vmcnt(6)\n"
    "s_add_u32 m0, s98, 0xc00\n"
    "s_mov_b32 s73, 0x15180\n"
    "s_nop 0\n"
    "buffer_load_dwordx4 v7, s[44:47], s73 offen offset:0 lds\n"
    "v_dot2c_f32_bf16 v36, s70, v220\n"
    "v_dot2c_f32_bf16 v37, s71, v220\n"
    "v_dot2c_f32_bf16 v38, s70, v221\n"
    "v_dot2c_f32_bf16 v39, s71, v221\n"
    "v_dot2c_f32_bf16 v40, s70, v222\n"
    "v_dot2c_f32_bf16 v41, s71, v222\n"
    "v_dot2c_f32_bf16 v42, s70, v223\n"
    "v_dot2c_f32_bf16 v43, s71, v223\n"
    "s_waitcnt vmcnt(6)\n"
    "s_add_u32 m0, s98, 0x1000\n"
    "s_mov_b32 s73, 0x1c200\n"
    "s_nop 0\n"
    "buffer_load_dwordx4 v7, s[44:47], s73 offen offset:0 lds\n"
    "v_dot2c_f32_bf16 v36, s70, v224\n"
    "v_dot2c_f32_bf16 v37, s71, v224\n"
    "v_dot2c_f32_bf16 v38, s70, v225\n"
    "v_dot2c_f32_bf16 v39, s71, v225\n"
    "v_dot2c_f32_bf16 v40, s70, v226\n"
    "v_dot2c_f32_bf16 v41, s71, v226\n"
    "v_dot2c_f32_bf16 v42, s70, v227\n"
    "v_dot2c_f32_bf16 v43, s71, v227\n"
    "s_waitcnt vmcnt(6)\n"
    "s_add_u32 m0, s98, 0x1400\n"
    "s_mov_b32 s73, 0x23280\n"
    "s_nop 0\n"
    "buffer_load_dwordx4 v7, s[44:47], s73 offen offset:0 lds\n"
    "v_dot2c_f32_bf16 v36, s70, v228\n"
    "v_dot2c_f32_bf16 v37, s71, v228\n"
    "v_dot2c_f32_bf16 v38, s70, v229\n"
    "v_dot2c_f32_bf16 v39, s71, v229\n"
    "v_dot2c_f32_bf16 v40, s70, v230\n"
    "v_dot2c_f32_bf16 v41, s71, v230\n"
    "v_dot2c_f32_bf16 v42, s70, v231\n"
    "v_dot2c_f32_bf16 v43, s71, v231\n"
    "v_permlane32_swap_b32 v36, v40\n"
    "v_permlane32_swap_b32 v37, v41\n"
    "v_permlane32_swap_b32 v38, v42\n"
    "v_permlane32_swap_b32 v39, v43\n"
    "v_add_f32 v36, v36, v40\n"
    "v_add_f32 v37, v37, v41\n"
    "v_add_f32 v38, v38, v42\n"
    "v_add_f32 v39, v39, v43\n"
    "s_nop 0\n"
    "v_permlane16_swap_b32 v36, v38\n"
    "v_permlane16_swap_b32 v37, v39\n"
    "v_add_f32 v36, v36, v38\n"
    "v_add_f32 v37, v37, v39\n"
    "v_and_b32 v9, 7, v0\n"
    "v_lshlrev_b32 v9, 5, v9\n"
    "v_lshrrev_b32 v10, 4, v0\n"
    "v_add_f32_dpp v36, v36, v36 row_ror:8 row_mask:0xf bank_mask:0xf\n"
    "v_add_f32_dpp v37, v37, v37 row_ror:8 row_mask:0xf bank_mask:0xf\n"
    "v_lshl_add_u32 v9, v10, 3, v9\n"
    "s_lshl_b32 s62, s60, 8\n"
    "v_add_u32 v9, s62, v9\n"
    "ds_write_b64 v9, v[36:37] offset:0\n"
    "v_lshlrev_b32 v9, 2, v0\n"
    "s_lshl_b32 s62, s61, 10\n"
    "s_add_u32 s62, s62, 106496\n"
    "v_lshl_add_u32 v10, v0, 4, s62\n"
    "s_cmp_lt_u32 s61, 28\ns_cselect_b32 s62, s60, 1\n"
    "s_mov_b32 s63, 0\n"
    "s_cmp_eq_u32 s62, 0\ns_cselect_b32 s62, s63, 0x70000000\n"
    "v_add_u32 v10, s62, v10\n"
    "s_waitcnt lgkmcnt(0)\n"
    "s_add_u32 m0, s98, 0x1800\n"
    "s_mov_b32 s73, 0x2a300\n"
    "s_nop 0\n"
    "buffer_load_dwordx4 v7, s[44:47], s73 offen offset:0 lds\n"
    "s_barrier\n"
    "s_add_u32 m0, s98, 0x1c00\n"
    "s_mov_b32 s73, 0x31380\n"
    "s_nop 0\n"
    "buffer_load_dwordx4 v7, s[44:47], s73 offen offset:0 lds\n"
    "ds_read_b32 v13, v9 offset:0\n"
    "ds_read_b32 v14, v9 offset:256\n"
    "ds_read_b32 v15, v9 offset:512\n"
    "s_add_u32 m0, s98, 0x8000\n"
    "s_mov_b32 s73, 0x0\n"
    "s_nop 0\n"
    "buffer_load_dwordx4 v7, s[48:51], s73 offen offset:0 lds\n"
    "ds_read_b32 v16, v9 offset:768\n"
    "s_waitcnt lgkmcnt(0)\n"
    "s_add_u32 m0, s98, 0x8400\n"
    "s_mov_b32 s73, 0x7080\n"
    "s_nop 0\n"
    "buffer_load_dwordx4 v7, s[48:51], s73 offen offset:0 lds\n"
    "v_add_f32 v13, v13, v14\n"
    "v_add_f32 v15, v15, v16\n"
    "v_cvt_pk_bf16_f32 v14, v13, v15\n"
    "v_lshlrev_b32 v17, 16, v14\n"
    "v_and_b32 v18, 0xffff0000, v14\n"
    "v_sub_f32 v13, v13, v17\n"
    "v_sub_f32 v15, v15, v18\n"
    "v_cvt_pk_bf16_f32 v32, v17, v13\n"
    "v_cvt_pk_bf16_f32 v34, v18, v15\n"
    "v_mov_b32 v33, s74\n"
    "v_mov_b32 v35, s75\n"
    "buffer_store_dwordx4 v[32:35], v10, s[52:55], 0 offen offset:0 sc1\n"
    "v_lshlrev_b32 v6, 4, v0\n"
    "ds_read_b128 a[0:3], v6 offset:4096\n"
    "ds_read_b128 a[4:7], v6 offset:5120\n"
    "ds_read_b128 a[8:11], v6 offset:6144\n"
    "s_add_u32 m0, s98, 0x8800\n"
    "s_mov_b32 s73, 0xe100\n"
    "s_nop 0\n"
    "buffer_load_dwordx4 v7, s[48:51], s73 offen offset:0 lds\n"
    "ds_read_b128 a[12:15], v6 offset:7168\n"
    "ds_read_b128 a[16:19], v6 offset:8192\n"
    "ds_read_b128 a[20:23], v6 offset:9216\n"
    "ds_read_b128 a[24:27], v6 offset:10240\n"
    "ds_read_b128 a[28:31], v6 offset:11264\n"
    "ds_read_b128 a[32:35], v6 offset:12288\n"
    "ds_read_b128 a[36:39], v6 offset:13312\n"
    "ds_read_b128 a[40:43], v6 offset:14336\n"
    "ds_read_b128 a[44:47], v6 offset:15360\n"
    "ds_read_b128 a[48:51], v6 offset:16384\n"
    "s_add_u32 m0, s98, 0x8c00\n"
    "s_mov_b32 s73, 0x15180\n"
    "s_nop 0\n"
    "buffer_load_dwordx4 v7, s[48:51], s73 offen offset:0 lds\n"
    "ds_read_b128 a[52:55], v6 offset:17408\n"
    "ds_read_b128 a[56:59], v6 offset:18432\n"
    "s_waitcnt lgkmcnt(13)\n"
    "ds_read_b128 a[60:63], v6 offset:19456\n"
    "ds_read_b128 a[64:67], v6 offset:20480\n"
    "s_waitcnt lgkmcnt(13)\n"
    "ds_read_b128 a[68:71], v6 offset:21504\n"
    "ds_read_b128 a[72:75], v6 offset:22528\n"
    "s_waitcnt lgkmcnt(13)\n"
    "ds_read_b128 a[76:79], v6 offset:23552\n"
    "s_add_u32 m0, s98, 0x9000\n"
    "s_mov_b32 s73, 0x1c200\n"
    "s_nop 0\n"
    "buffer_load_dwordx4 v7, s[48:51], s73 offen offset:0 lds\n"
    "ds_read_b128 a[80:83], v6 offset:24576\n"
    "s_waitcnt lgkmcnt(13)\n"
    "ds_read_b128 a[84:87], v6 offset:25600\n"
    "ds_read_b128 a[88:91], v6 offset:26624\n"
    "s_waitcnt lgkmcnt(13)\n"
    "ds_read_b128 a[92:95], v6 offset:27648\n"
    "s_add_u32 m0, s98, 0x9400\n"
    "s_mov_b32 s73, 0x23280\n"
    "s_nop 0\n"
    "buffer_load_dwordx4 v7, s[48:51], s73 offen offset:0 lds\n"
    "s_add_u32 m0, s98, 0x9800\n"
    "s_mov_b32 s73, 0x2a300\n"
    "s_nop 0\n"
    "buffer_load_dwordx4 v7, s[48:51], s73 offen offset:0 lds\n"
    "s_add_u32 m0, s98, 0x9c00\n"
    "s_mov_b32 s73, 0x31380\n"
    "s_nop 0\n"
    "buffer_load_dwordx4 v7, s[48:51], s73 offen offset:0 lds\n"
    "v_mov_b32 v19, s60\n"
    "v_lshlrev_b32 v9, 9, v0\n"
    "s_and_b32 s62, s61, 7\n"
    "s_lshl_b32 s63, s60, 6\n"
    "s_add_u32 s62, s62, s63\n"
    "v_add_lshl_u32 v10, s62, v1, 1\n"
    "v_lshl_add_u32 v165, v2, 12, v10\n"
    "v_add_u32 v164, 64, v165\n"
    "v_and_b32 v10, 1, v1\n"
    "v_lshlrev_b32 v10, 1, v10\n"
    "v_lshl_add_u32 v10, v2, 12, v10\n"
    "v_add_u32 v10, 480, v10\n"
    "v_cmp_eq_u32 vcc, 3, v19\n"
    "v_add_u32 v165, 65536, v165\n"
    "s_nop 0\n"
    "v_cndmask_b32 v164, v164, v10, vcc\n"
    "v_add_u32 v164, 65536, v164\n"
    "s_mul_i32 s62, s61, 66816\n"
    "s_lshl_b32 s63, s60, 14\n"
    "s_add_u32 s62, s62, s63\n"
    "s_add_u32 s62, s62, 1048576\n"
    "v_lshrrev_b32 v11, 4, v1\n"
    "v_lshlrev_b32 v11, 12, v11\n"
    "v_lshl_add_u32 v11, v2, 9, v11\n"
    "v_and_b32 v12, 15, v1\n"
    "v_lshl_add_u32 v11, v12, 3, v11\n"
    "v_add_u32 v4, s62, v11\n"
    "v_add_u32 v3, 8192, v4\n"
    "v_lshl_add_u32 v11, s60, 5, v1\n"
    "s_add_u32 m0, s64, 0\n"
    "s_mov_b32 s73, 0\n"
    "s_nop 0\n"
    "buffer_load_dwordx4 v214, s[56:59], s73 offen offset:0 lds\n"
    "v_cmp_lt_u32 vcc, 111, v11\n"
    "v_mov_b32 v11, 0x7ffffff0\n"
    "s_nop 0\n"
    "v_cndmask_b32 v3, v3, v11, vcc\n"
    "v_lshrrev_b32 v11, 4, v0\n"
    "s_lshl_b32 s62, s60, 2\n"
    "v_add_u32 v11, s62, v11\n"
    "v_cmp_gt_u32 vcc, 14, v11\n"
    "v_and_b32 v12, 15, v0\n"
    "v_mul_u32_u24 v12, 14, v12\n"
    "v_cndmask_b32 v11, 0, v11, vcc\n"
    "v_add_lshl_u32 v11, v11, v12, 9\n"
    "s_lshr_b32 s62, s61, 3\n"
    "s_lshl_b32 s62, s62, 7\n"
    "s_add_u32 s62, s62, 655360\n"
    "v_add_u32 v5, s62, v11\n"
    "v_lshrrev_b32 v11, 3, v0\n"
    "v_and_b32 v12, 3, v11\n"
    "v_lshrrev_b32 v13, 5, v0\n"
    "v_lshl_add_u32 v12, v13, 3, v12\n"
    "v_mul_u32_u24 v12, 34048, v12\n"
    "v_and_b32 v13, 7, v0\n"
    "v_lshl_add_u32 v12, v13, 4, v12\n"
    "s_lshl_b32 s62, s61, 10\n"
    "s_lshl_b32 s63, s60, 8\n"
    "s_add_u32 s62, s62, s63\n"
    "s_add_u32 s62, s62, 3670016\n"
    "v_add_u32 v8, s62, v12\n"
    "v_xor_b32 v13, v13, v11\n"
    "v_lshlrev_b32 v13, 4, v13\n"
    "v_lshl_add_u32 v13, v11, 7, v13\n"
    "s_add_u32 m0, s64, 1024\n"
    "s_mov_b32 s73, 4096\n"
    "s_nop 0\n"
    "buffer_load_dwordx4 v214, s[56:59], s73 offen offset:0 lds\n"
    "s_lshl_b32 s62, s60, 13\n"
    "s_add_u32 s62, s62, 32768\n"
    "v_add_u32 v7, s62, v13\n"
    "v_and_b32 v11, 7, v1\n"
    "v_xor_b32 v11, v11, v2\n"
    "v_lshlrev_b32 v11, 4, v11\n"
    "v_lshl_add_u32 v11, v1, 7, v11\n"
    "v_add_u32 v6, s62, v11\n"
    "s_mov_b32 s67, 0x05040100\n"
    "s_mov_b32 s68, 0x07060302\n"
    "s_mov_b32 s69, 0xffff0000\n"
    "v_add_u32 v9, 65536, v9\n"
    "s_and_b32 s62, s61, 7\n"
    "s_lshl_b32 s62, s62, 1\n"
    "v_add_u32 v10, s62, v9\n"
    "s_lshl_b32 s62, s60, 7\n"
    "v_lshl_add_u32 v11, v0, 1, s62\n"
    "s_waitcnt vmcnt(19)\n"
    "ds_write_b16 v11, v167 offset:1024\n"
    "v_lshlrev_b32 v11, 1, v0\n"
    "s_add_u32 m0, s64, 2048\n"
    "s_mov_b32 s73, 8192\n"
    "s_nop 0\n"
    "buffer_load_dwordx4 v214, s[56:59], s73 offen offset:0 lds\n"
    "s_add_u32 m0, s64, 3072\n"
    "s_mov_b32 s73, 12288\n"
    "s_nop 0\n"
    "buffer_load_dwordx4 v214, s[56:59], s73 offen offset:0 lds\n"
    "s_add_u32 m0, s64, 4096\n"
    "s_mov_b32 s73, 16384\n"
    "s_nop 0\n"
    "buffer_load_dwordx4 v214, s[56:59], s73 offen offset:0 lds\n"
    "s_add_u32 m0, s64, 5120\n"
    "s_mov_b32 s73, 20480\n"
    "s_nop 0\n"
    "buffer_load_dwordx4 v214, s[56:59], s73 offen offset:0 lds\n"
    "s_add_u32 m0, s64, 6144\n"
    "s_mov_b32 s73, 24576\n"
    "s_nop 0\n"
    "buffer_load_dwordx4 v214, s[56:59], s73 offen offset:0 lds\n"
    "s_waitcnt vmcnt(7) lgkmcnt(0)\n"
    "s_add_u32 m0, s64, 7168\n"
    "s_mov_b32 s73, 28672\n"
    "s_nop 0\n"
    "buffer_load_dwordx4 v214, s[56:59], s73 offen offset:0 lds\n"
    "s_barrier\n"
    "ds_read_u16 v20, v10 offset:0\n"
    "ds_read_u16 v21, v10 offset:32768\n"
    "ds_read_u16 v22, v10 offset:448\n"
    "ds_read_u16 v23, v10 offset:33216\n"
    "ds_read_u16 v24, v11 offset:1024\n"
    "ds_read_u16 v25, v11 offset:1152\n"
    "ds_read_u16 v26, v11 offset:1280\n"
    "s_add_u32 m0, s64, 0\n"
    "s_mov_b32 s73, 32768\n"
    "s_nop 0\n"
    "buffer_load_dwordx4 v214, s[56:59], s73 offen offset:0 lds\n"
    "ds_read_u16 v27, v11 offset:1408\n"
    "s_add_u32 m0, s64, 1024\n"
    "s_mov_b32 s73, 36864\n"
    "s_nop 0\n"
    "buffer_load_dwordx4 v214, s[56:59], s73 offen offset:0 lds\n"
    "s_waitcnt lgkmcnt(0)\n"
    "v_lshlrev_b32 v24, 16, v24\n"
    "v_lshlrev_b32 v25, 16, v25\n"
    "v_lshlrev_b32 v26, 16, v26\n"
    "v_lshlrev_b32 v27, 16, v27\n"
    "v_lshlrev_b32 v20, 16, v20\n"
    "v_lshlrev_b32 v21, 16, v21\n"
    "v_lshlrev_b32 v22, 16, v22\n"
    "v_lshlrev_b32 v23, 16, v23\n"
    "v_add_f32 v20, v20, v24\n"
    "v_sub_f32 v21, v21, v25\n"
    "v_add_f32 v22, v22, v26\n"
    "v_sub_f32 v23, v23, v27\n"
    "v_mul_f32 v20, 0x3e800000, v20\n"
    "v_mul_f32 v21, 0x3e800000, v21\n"
    "v_mul_f32 v22, 0x3e800000, v22\n"
    "v_mul_f32 v23, 0x3e800000, v23\n"
    "v_cvt_pk_bf16_f32 v24, v20, -v23\n"
    "v_cvt_pk_bf16_f32 v25, v21, v22\n"
    "s_add_u32 m0, s64, 2048\n"
    "s_mov_b32 s73, 40960\n"
    "s_nop 0\n"
    "buffer_load_dwordx4 v214, s[56:59], s73 offen offset:0 lds\n"
    "s_mov_b32 s62, 0\n"
    "s_cmp_eq_u32 s60, 3\ns_cselect_b32 s62, s62, 16\n"
    "v_add_u32 v9, s62, v9\n"
    "ds_write_b32 v9, v24 offset:480\n"
    "ds_write_b32 v9, v25 offset:33248\n"
    "v_mov_b32 v216, 0\n"
    "v_mov_b32 v217, 0\n"
    "v_mov_b32 v218, 0\n"
    "v_mov_b32 v219, 0\n"
    "v_mov_b32 v220, 0\n"
    "v_mov_b32 v221, 0\n"
    "v_mov_b32 v222, 0\n"
    "v_mov_b32 v223, 0\n"
    "v_mov_b32 v224, 0\n"
    "v_mov_b32 v225, 0\n"
    "v_mov_b32 v226, 0\n"
    "v_mov_b32 v227, 0\n"
    "v_mov_b32 v228, 0\n"
    "v_mov_b32 v229, 0\n"
    "v_mov_b32 v230, 0\n"
    "v_mov_b32 v231, 0\n"
    "v_mov_b32 v232, 0\n"
    "v_mov_b32 v233, 0\n"
    "v_mov_b32 v234, 0\n"
    "v_mov_b32 v235, 0\n"
    "v_mov_b32 v236, 0\n"
    "s_add_u32 m0, s64, 3072\n"
    "s_mov_b32 s73, 45056\n"
    "s_nop 0\n"
    "buffer_load_dwordx4 v214, s[56:59], s73 offen offset:0 lds\n"
    "v_mov_b32 v237, 0\n"
    "v_mov_b32 v238, 0\n"
    "v_mov_b32 v239, 0\n"
    "v_mov_b32 v240, 0\n"
    "v_mov_b32 v241, 0\n"
    "v_mov_b32 v242, 0\n"
    "v_mov_b32 v243, 0\n"
    "v_mov_b32 v244, 0\n"
    "v_mov_b32 v245, 0\n"
    "v_mov_b32 v246, 0\n"
    "v_mov_b32 v247, 0\n"
    "v_mov_b32 v148, 0\n"
    "v_mov_b32 v149, 0\n"
    "v_mov_b32 v150, 0\n"
    "v_mov_b32 v151, 0\n"
    "v_mov_b32 v152, 0\n"
    "v_mov_b32 v153, 0\n"
    "v_mov_b32 v154, 0\n"
    "v_mov_b32 v155, 0\n"
    "v_mov_b32 v156, 0\n"
    "v_mov_b32 v157, 0\n"
    "v_mov_b32 v158, 0\n"
    "v_mov_b32 v159, 0\n"
    "v_mov_b32 v160, 0\n"
    "v_mov_b32 v161, 0\n"
    "v_mov_b32 v162, 0\n"
    "s_add_u32 m0, s64, 4096\n"
    "s_mov_b32 s73, 49152\n"
    "s_nop 0\n"
    "buffer_load_dwordx4 v214, s[56:59], s73 offen offset:0 lds\n"
    "v_mov_b32 v163, 0\n"
    "v_mov_b32 v166, 0\n"
    "v_mov_b32 v167, 0\n"
    "v_mov_b32 v168, 0\n"
    "v_mov_b32 v169, 0\n"
    "v_mov_b32 v170, 0\n"
    "v_mov_b32 v171, 0\n"
    "v_mov_b32 v172, 0\n"
    "v_mov_b32 v173, 0\n"
    "v_mov_b32 v174, 0\n"
    "v_mov_b32 v175, 0\n"
    "v_mov_b32 v176, 0\n"
    "v_mov_b32 v177, 0\n"
    "v_mov_b32 v178, 0\n"
    "v_mov_b32 v179, 0\n"
    "v_mov_b32 v212, 0\n"
    "v_mov_b32 v213, 0\n"
    "s_mov_b32 s62, 0x3f800000\n"
    "s_cmp_eq_u32 s60, 3\ns_cselect_b32 s72, s62, 0\n"
    "ds_read_u16_d16_hi v216, v164 offset:0\n"
    "ds_read_u16_d16_hi v220, v164 offset:32768\n"
    "ds_read_u16_d16_hi v217, v164 offset:512\n"
    "ds_read_u16_d16_hi v221, v164 offset:33280\n"
    "ds_read_u16_d16_hi v218, v164 offset:1024\n"
    "ds_read_u16_d16_hi v222, v164 offset:33792\n"
    "ds_read_u16_d16_hi v219, v164 offset:1536\n"
    "ds_read_u16_d16_hi v223, v164 offset:34304\n"
    "ds_read_u16_d16_hi v224, v164 offset:2048\n"
    "ds_read_u16_d16_hi v228, v164 offset:34816\n"
    "ds_read_u16_d16_hi v225, v164 offset:2560\n"
    "ds_read_u16_d16_hi v229, v164 offset:35328\n"
    "ds_read_u16_d16_hi v226, v164 offset:3072\n"
    "s_waitcnt lgkmcnt(13)\n"
    "ds_read_u16_d16_hi v230, v164 offset:35840\n"
    "ds_read_u16_d16_hi v227, v164 offset:3584\n"
    "s_waitcnt lgkmcnt(13)\n"
    "ds_read_u16_d16_hi v231, v164 offset:36352\n"
    "ds_read_u16_d16_hi v232, v164 offset:8192\n"
    "s_waitcnt lgkmcnt(13)\n"
    "ds_read_u16_d16_hi v236, v164 offset:40960\n"
    "ds_read_u16_d16_hi v233, v164 offset:8704\n"
    "s_waitcnt lgkmcnt(13)\n"
    "ds_read_u16_d16_hi v237, v164 offset:41472\n"
    "ds_read_u16_d16_hi v234, v164 offset:9216\n"
    "s_waitcnt lgkmcnt(13)\n"
    "ds_read_u16_d16_hi v238, v164 offset:41984\n"
    "ds_read_u16_d16_hi v235, v164 offset:9728\n"
    "s_waitcnt lgkmcnt(13)\n"
    "ds_read_u16_d16_hi v239, v164 offset:42496\n"
    "ds_read_u16_d16_hi v240, v164 offset:10240\n"
    "s_waitcnt lgkmcnt(13)\n"
    "ds_read_u16_d16_hi v244, v164 offset:43008\n"
    "ds_read_u16_d16_hi v241, v164 offset:10752\n"
    "s_waitcnt lgkmcnt(13)\n"
    "ds_read_u16_d16_hi v245, v164 offset:43520\n"
    "ds_read_u16_d16_hi v242, v164 offset:11264\n"
    "s_waitcnt lgkmcnt(13)\n"
    "ds_read_u16_d16_hi v246, v164 offset:44032\n"
    "ds_read_u16_d16_hi v243, v164 offset:11776\n"
    "s_waitcnt lgkmcnt(13)\n"
    "ds_read_u16_d16_hi v247, v164 offset:44544\n"
    "ds_read_u16_d16_hi v148, v164 offset:16384\n"
    "s_waitcnt lgkmcnt(13)\n"
    "ds_read_u16_d16_hi v152, v164 offset:49152\n"
    "ds_read_u16_d16_hi v149, v164 offset:16896\n"
    "s_waitcnt lgkmcnt(13)\n"
    "ds_read_u16_d16_hi v153, v164 offset:49664\n"
    "ds_read_u16_d16_hi v150, v164 offset:17408\n"
    "s_waitcnt lgkmcnt(13)\n"
    "ds_read_u16_d16_hi v154, v164 offset:50176\n"
    "ds_read_u16_d16_hi v151, v164 offset:17920\n"
    "s_waitcnt lgkmcnt(13)\n"
    "ds_read_u16_d16_hi v155, v164 offset:50688\n"
    "ds_read_u16_d16_hi v156, v164 offset:18432\n"
    "s_waitcnt lgkmcnt(13)\n"
    "ds_read_u16_d16_hi v160, v164 offset:51200\n"
    "ds_read_u16_d16_hi v157, v164 offset:18944\n"
    "s_waitcnt lgkmcnt(13)\n"
    "ds_read_u16_d16_hi v161, v164 offset:51712\n"
    "ds_read_u16_d16_hi v158, v164 offset:19456\n"
    "s_waitcnt lgkmcnt(13)\n"
    "ds_read_u16_d16_hi v162, v164 offset:52224\n"
    "ds_read_u16_d16_hi v159, v164 offset:19968\n"
    "s_waitcnt lgkmcnt(13)\n"
    "ds_read_u16_d16_hi v163, v164 offset:52736\n"
    "ds_read_u16_d16_hi v166, v164 offset:24576\n"
    "s_waitcnt lgkmcnt(13)\n"
    "ds_read_u16_d16_hi v170, v164 offset:57344\n"
    "ds_read_u16_d16_hi v167, v164 offset:25088\n"
    "s_waitcnt lgkmcnt(13)\n"
    "ds_read_u16_d16_hi v171, v164 offset:57856\n"
    "ds_read_u16_d16_hi v168, v164 offset:25600\n"
    "s_waitcnt lgkmcnt(13)\n"
    "ds_read_u16_d16_hi v172, v164 offset:58368\n"
    "ds_read_u16_d16_hi v169, v164 offset:26112\n"
    "s_waitcnt lgkmcnt(13)\n"
    "ds_read_u16_d16_hi v173, v164 offset:58880\n"
    "ds_read_u16_d16_hi v174, v164 offset:26624\n"
    "s_waitcnt lgkmcnt(13)\n"
    "ds_read_u16_d16_hi v178, v164 offset:59392\n"
    "ds_read_u16_d16_hi v175, v164 offset:27136\n"
    "s_waitcnt lgkmcnt(13)\n"
    "ds_read_u16_d16_hi v179, v164 offset:59904\n"
    "ds_read_u16_d16_hi v176, v164 offset:27648\n"
    "s_waitcnt lgkmcnt(13)\n"
    "ds_read_u16_d16_hi v212, v164 offset:60416\n"
    "ds_read_u16_d16_hi v177, v164 offset:28160\n"
    "s_waitcnt lgkmcnt(13)\n"
    "ds_read_u16_d16_hi v213, v164 offset:60928\n"
    "s_mov_b32 s66, 32768\n"
    ".Lp_t_loop:\n"
    "buffer_load_dwordx2 v[250:251], v5, s[52:55], 0 offen offset:0 sc1\n"
    "s_waitcnt vmcnt(0)\n"
    "v_cmp_ne_u64 vcc, s[74:75], v[250:251]\n"
    "s_nop 3\n"
    "s_cbranch_vccz .Lp_t_ok\n"
    "s_sleep 1\n"
    "s_sub_u32 s66, s66, 1\n"
    "s_cmp_lg_u32 s66, 0\n"
    "s_cbranch_scc1 .Lp_t_loop\n"
    ".Lp_t_ok:\n"
    "buffer_load_dwordx2 a[96:97], v3, s[52:55], 0 offen offset:0 sc1\n"
    "buffer_load_dwordx2 a[98:99], v3, s[52:55], 0 offen offset:256 sc1\n"
    "buffer_load_dwordx2 a[100:101], v3, s[52:55], 0 offen offset:128 sc1\n"
    "buffer_load_dwordx2 a[102:103], v3, s[52:55], 0 offen offset:384 sc1\n"
    "buffer_load_dwordx2 a[104:105], v3, s[52:55], 0 offen offset:1024 sc1\n"
    "buffer_load_dwordx2 a[106:107], v3, s[52:55], 0 offen offset:1280 sc1\n"
    "buffer_load_dwordx2 a[108:109], v3, s[52:55], 0 offen offset:1152 sc1\n"
    "buffer_load_dwordx2 a[110:111], v3, s[52:55], 0 offen offset:1408 sc1\n"
    "buffer_load_dwordx2 a[112:113], v3, s[52:55], 0 offen offset:2048 sc1\n"
    "buffer_load_dwordx2 a[114:115], v3, s[52:55], 0 offen offset:2304 sc1\n"
    "buffer_load_dwordx2 a[116:117], v3, s[52:55], 0 offen offset:2176 sc1\n"
    "buffer_load_dwordx2 a[118:119], v3, s[52:55], 0 offen offset:2432 sc1\n"
    "buffer_load_dwordx2 a[120:121], v3, s[52:55], 0 offen offset:3072 sc1\n"
    "buffer_load_dwordx2 a[122:123], v3, s[52:55], 0 offen offset:3328 sc1\n"
    "buffer_load_dwordx2 a[124:125], v3, s[52:55], 0 offen offset:3200 sc1\n"
    "buffer_load_dwordx2 a[126:127], v3, s[52:55], 0 offen offset:3456 sc1\n"
    "s_waitcnt vmcnt(14)\n"
    "v_mfma_f32_32x32x16_bf16 v[20:35], a[0:3], a[96:99], 0\n"
    "buffer_load_dwordx2 a[128:129], v4, s[52:55], 0 offen offset:0 sc1\n"
    "buffer_load_dwordx2 a[130:131], v4, s[52:55], 0 offen offset:256 sc1\n"
    "v_mfma_f32_32x32x16_bf16 v[36:51], a[64:67], a[96:99], 0\n"
    "buffer_load_dwordx2 a[132:133], v4, s[52:55], 0 offen offset:128 sc1\n"
    "s_waitcnt vmcnt(15)\n"
    "v_mfma_f32_32x32x16_bf16 v[36:51], a[0:3], a[100:103], v[36:51]\n"
    "buffer_load_dwordx2 a[134:135], v4, s[52:55], 0 offen offset:384 sc1\n"
    "v_mfma_f32_32x32x16_bf16 v[20:35], a[32:35], a[100:103], v[20:35]\n"
    "buffer_load_dwordx2 a[136:137], v4, s[52:55], 0 offen offset:1024 sc1\n"
    "v_mfma_f32_32x32x16_bf16 v[52:67], a[16:19], a[96:99], 0\n"
    "v_mfma_f32_32x32x16_bf16 v[68:83], a[80:83], a[96:99], 0\n"
    "buffer_load_dwordx2 a[138:139], v4, s[52:55], 0 offen offset:1280 sc1\n"
    "v_mfma_f32_32x32x16_bf16 v[68:83], a[16:19], a[100:103], v[68:83]\n"
    "v_mfma_f32_32x32x16_bf16 v[52:67], a[48:51], a[100:103], v[52:67]\n"
    "buffer_load_dwordx2 a[140:141], v4, s[52:55], 0 offen offset:1152 sc1\n"
    "s_waitcnt vmcnt(17)\n"
    "v_mfma_f32_32x32x16_bf16 v[20:35], a[4:7], a[104:107], v[20:35]\n"
    "v_mfma_f32_32x32x16_bf16 v[36:51], a[68:71], a[104:107], v[36:51]\n"
    "buffer_load_dwordx2 a[142:143], v4, s[52:55], 0 offen offset:1408 sc1\n"
    "s_waitcnt vmcnt(16)\n"
    "v_mfma_f32_32x32x16_bf16 v[36:51], a[4:7], a[108:111], v[36:51]\n"
    "v_mfma_f32_32x32x16_bf16 v[20:35], a[36:39], a[108:111], v[20:35]\n"
    "buffer_load_dwordx2 a[144:145], v4, s[52:55], 0 offen offset:2048 sc1\n"
    "v_mfma_f32_32x32x16_bf16 v[52:67], a[20:23], a[104:107], v[52:67]\n"
    "buffer_load_dwordx2 a[146:147], v4, s[52:55], 0 offen offset:2304 sc1\n"
    "v_mfma_f32_32x32x16_bf16 v[68:83], a[84:87], a[104:107], v[68:83]\n"
    "v_mfma_f32_32x32x16_bf16 v[68:83], a[20:23], a[108:111], v[68:83]\n"
    "buffer_load_dwordx2 a[148:149], v4, s[52:55], 0 offen offset:2176 sc1\n"
    "v_mfma_f32_32x32x16_bf16 v[52:67], a[52:55], a[108:111], v[52:67]\n"
    "s_waitcnt vmcnt(17)\n"
    "v_mfma_f32_32x32x16_bf16 v[20:35], a[8:11], a[112:115], v[20:35]\n"
    "buffer_load_dwordx2 a[150:151], v4, s[52:55], 0 offen offset:2432 sc1\n"
    "v_mfma_f32_32x32x16_bf16 v[36:51], a[72:75], a[112:115], v[36:51]\n"
    "s_waitcnt vmcnt(16)\n"
    "v_mfma_f32_32x32x16_bf16 v[36:51], a[8:11], a[116:119], v[36:51]\n"
    "buffer_load_dwordx2 a[152:153], v4, s[52:55], 0 offen offset:3072 sc1\n"
    "v_mfma_f32_32x32x16_bf16 v[20:35], a[40:43], a[116:119], v[20:35]\n"
    "buffer_load_dwordx2 a[154:155], v4, s[52:55], 0 offen offset:3328 sc1\n"
    "v_mfma_f32_32x32x16_bf16 v[52:67], a[24:27], a[112:115], v[52:67]\n"
    "v_mfma_f32_32x32x16_bf16 v[68:83], a[88:91], a[112:115], v[68:83]\n"
    "buffer_load_dwordx2 a[156:157], v4, s[52:55], 0 offen offset:3200 sc1\n"
    "v_mfma_f32_32x32x16_bf16 v[68:83], a[24:27], a[116:119], v[68:83]\n"
    "v_mfma_f32_32x32x16_bf16 v[52:67], a[56:59], a[116:119], v[52:67]\n"
    "buffer_load_dwordx2 a[158:159], v4, s[52:55], 0 offen offset:3456 sc1\n"
    "s_waitcnt vmcnt(18)\n"
    "v_mfma_f32_32x32x16_bf16 v[20:35], a[12:15], a[120:123], v[20:35]\n"
    "v_mfma_f32_32x32x16_bf16 v[36:51], a[76:79], a[120:123], v[36:51]\n"
    "s_waitcnt vmcnt(16)\n"
    "v_mfma_f32_32x32x16_bf16 v[36:51], a[12:15], a[124:127], v[36:51]\n"
    "v_mfma_f32_32x32x16_bf16 v[20:35], a[44:47], a[124:127], v[20:35]\n"
    "v_mfma_f32_32x32x16_bf16 v[52:67], a[28:31], a[120:123], v[52:67]\n"
    "v_mfma_f32_32x32x16_bf16 v[68:83], a[92:95], a[120:123], v[68:83]\n"
    "v_mfma_f32_32x32x16_bf16 v[68:83], a[28:31], a[124:127], v[68:83]\n"
    "v_mfma_f32_32x32x16_bf16 v[52:67], a[60:63], a[124:127], v[52:67]\n"
    "s_waitcnt vmcnt(14)\n"
    "v_mfma_f32_32x32x16_bf16 v[84:99], a[0:3], a[128:131], 0\n"
    "v_mfma_f32_32x32x16_bf16 v[100:115], a[64:67], a[128:131], 0\n"
    "s_nop 7\n"
    "v_mul_f32 v9, v20, v216\n"
    "v_mul_f32 v20, v20, v220\n"
    "v_fma_f32 v9, -v36, v220, v9\n"
    "v_fmac_f32 v20, v36, v216\n"
    "v_mul_f32 v10, v21, v217\n"
    "s_waitcnt vmcnt(12)\n"
    "v_mfma_f32_32x32x16_bf16 v[100:115], a[0:3], a[132:135], v[100:115]\n"
    "v_mul_f32 v21, v21, v221\n"
    "v_fma_f32 v10, -v37, v221, v10\n"
    "v_fmac_f32 v21, v37, v217\n"
    "v_cvt_pk_bf16_f32 v180, v9, v10\n"
    "v_cvt_pk_bf16_f32 v184, v20, v21\n"
    "v_mfma_f32_32x32x16_bf16 v[84:99], a[32:35], a[132:135], v[84:99]\n"
    "v_mul_f32 v11, v22, v218\n"
    "v_mul_f32 v22, v22, v222\n"
    "v_fma_f32 v11, -v38, v222, v11\n"
    "v_fmac_f32 v22, v38, v218\n"
    "v_mul_f32 v12, v23, v219\n"
    "v_mfma_f32_32x32x16_bf16 v[116:131], a[16:19], a[128:131], 0\n"
    "v_mul_f32 v23, v23, v223\n"
    "v_fma_f32 v12, -v39, v223, v12\n"
    "v_fmac_f32 v23, v39, v219\n"
    "v_cvt_pk_bf16_f32 v181, v11, v12\n"
    "v_cvt_pk_bf16_f32 v185, v22, v23\n"
    "v_mfma_f32_32x32x16_bf16 v[132:147], a[80:83], a[128:131], 0\n"
    "v_mul_f32 v9, v24, v224\n"
    "v_mul_f32 v24, v24, v228\n"
    "v_fma_f32 v9, -v40, v228, v9\n"
    "v_fmac_f32 v24, v40, v224\n"
    "v_mul_f32 v10, v25, v225\n"
    "v_mfma_f32_32x32x16_bf16 v[132:147], a[16:19], a[132:135], v[132:147]\n"
    "v_mul_f32 v25, v25, v229\n"
    "v_fma_f32 v10, -v41, v229, v10\n"
    "v_fmac_f32 v25, v41, v225\n"
    "v_cvt_pk_bf16_f32 v182, v9, v10\n"
    "v_cvt_pk_bf16_f32 v186, v24, v25\n"
    "v_mfma_f32_32x32x16_bf16 v[116:131], a[48:51], a[132:135], v[116:131]\n"
    "v_mul_f32 v11, v26, v226\n"
    "v_mul_f32 v26, v26, v230\n"
    "v_fma_f32 v11, -v42, v230, v11\n"
    "v_fmac_f32 v26, v42, v226\n"
    "v_mul_f32 v12, v27, v227\n"
    "s_waitcnt vmcnt(10)\n"
    "v_mfma_f32_32x32x16_bf16 v[84:99], a[4:7], a[136:139], v[84:99]\n"
    "v_mul_f32 v27, v27, v231\n"
    "v_fma_f32 v12, -v43, v231, v12\n"
    "v_fmac_f32 v27, v43, v227\n"
    "v_cvt_pk_bf16_f32 v183, v11, v12\n"
    "v_cvt_pk_bf16_f32 v187, v26, v27\n"
    "v_mfma_f32_32x32x16_bf16 v[100:115], a[68:71], a[136:139], v[100:115]\n"
    "v_mul_f32 v9, v28, v232\n"
    "v_mul_f32 v28, v28, v236\n"
    "v_fma_f32 v9, -v44, v236, v9\n"
    "v_fmac_f32 v28, v44, v232\n"
    "v_mul_f32 v10, v29, v233\n"
    "s_waitcnt vmcnt(8)\n"
    "v_mfma_f32_32x32x16_bf16 v[100:115], a[4:7], a[140:143], v[100:115]\n"
    "v_mul_f32 v29, v29, v237\n"
    "v_fma_f32 v10, -v45, v237, v10\n"
    "v_fmac_f32 v29, v45, v233\n"
    "v_cvt_pk_bf16_f32 v188, v9, v10\n"
    "v_cvt_pk_bf16_f32 v192, v28, v29\n"
    "v_mfma_f32_32x32x16_bf16 v[84:99], a[36:39], a[140:143], v[84:99]\n"
    "v_mul_f32 v11, v30, v234\n"
    "v_mul_f32 v30, v30, v238\n"
    "v_fma_f32 v11, -v46, v238, v11\n"
    "v_fmac_f32 v30, v46, v234\n"
    "v_mul_f32 v12, v31, v235\n"
    "v_mfma_f32_32x32x16_bf16 v[116:131], a[20:23], a[136:139], v[116:131]\n"
    "v_mul_f32 v31, v31, v239\n"
    "v_fma_f32 v12, -v47, v239, v12\n"
    "v_fmac_f32 v31, v47, v235\n"
    "v_cvt_pk_bf16_f32 v189, v11, v12\n"
    "v_cvt_pk_bf16_f32 v193, v30, v31\n"
    "v_mfma_f32_32x32x16_bf16 v[132:147], a[84:87], a[136:139], v[132:147]\n"
    "v_mul_f32 v9, v32, v240\n"
    "v_mul_f32 v32, v32, v244\n"
    "v_fma_f32 v9, -v48, v244, v9\n"
    "v_fmac_f32 v32, v48, v240\n"
    "v_mul_f32 v10, v33, v241\n"
    "v_mfma_f32_32x32x16_bf16 v[132:147], a[20:23], a[140:143], v[132:147]\n"
    "v_mul_f32 v33, v33, v245\n"
    "v_fma_f32 v10, -v49, v245, v10\n"
    "v_fmac_f32 v33, v49, v241\n"
    "v_cvt_pk_bf16_f32 v190, v9, v10\n"
    "v_cvt_pk_bf16_f32 v194, v32, v33\n"
    "v_mfma_f32_32x32x16_bf16 v[116:131], a[52:55], a[140:143], v[116:131]\n"
    "v_mul_f32 v11, v34, v242\n"
    "v_mul_f32 v34, v34, v246\n"
    "v_fma_f32 v11, -v50, v246, v11\n"
    "v_fmac_f32 v34, v50, v242\n"
    "v_mul_f32 v12, v35, v243\n"
    "s_waitcnt vmcnt(6)\n"
    "v_mfma_f32_32x32x16_bf16 v[84:99], a[8:11], a[144:147], v[84:99]\n"
    "v_mul_f32 v35, v35, v247\n"
    "v_fma_f32 v12, -v51, v247, v12\n"
    "v_fmac_f32 v35, v51, v243\n"
    "v_cvt_pk_bf16_f32 v191, v11, v12\n"
    "v_cvt_pk_bf16_f32 v195, v34, v35\n"
    "v_mfma_f32_32x32x16_bf16 v[100:115], a[72:75], a[144:147], v[100:115]\n"
    "v_mul_f32 v9, v52, v148\n"
    "v_mul_f32 v52, v52, v152\n"
    "v_fma_f32 v9, -v68, v152, v9\n"
    "v_fmac_f32 v52, v68, v148\n"
    "v_mul_f32 v10, v53, v149\n"
    "s_waitcnt vmcnt(4)\n"
    "v_mfma_f32_32x32x16_bf16 v[100:115], a[8:11], a[148:151], v[100:115]\n"
    "v_mul_f32 v53, v53, v153\n"
    "v_fma_f32 v10, -v69, v153, v10\n"
    "v_fmac_f32 v53, v69, v149\n"
    "v_cvt_pk_bf16_f32 v196, v9, v10\n"
    "v_cvt_pk_bf16_f32 v200, v52, v53\n"
    "v_mfma_f32_32x32x16_bf16 v[84:99], a[40:43], a[148:151], v[84:99]\n"
    "v_mul_f32 v11, v54, v150\n"
    "v_mul_f32 v54, v54, v154\n"
    "v_fma_f32 v11, -v70, v154, v11\n"
    "v_fmac_f32 v54, v70, v150\n"
    "v_mul_f32 v12, v55, v151\n"
    "v_mfma_f32_32x32x16_bf16 v[116:131], a[24:27], a[144:147], v[116:131]\n"
    "v_mul_f32 v55, v55, v155\n"
    "v_fma_f32 v12, -v71, v155, v12\n"
    "v_fmac_f32 v55, v71, v151\n"
    "v_cvt_pk_bf16_f32 v197, v11, v12\n"
    "v_cvt_pk_bf16_f32 v201, v54, v55\n"
    "v_mfma_f32_32x32x16_bf16 v[132:147], a[88:91], a[144:147], v[132:147]\n"
    "ds_read_u16_d16_hi v148, v165 offset:0\n"
    "s_waitcnt lgkmcnt(13)\n"
    "ds_read_u16_d16_hi v152, v165 offset:32768\n"
    "ds_read_u16_d16_hi v149, v165 offset:512\n"
    "s_waitcnt lgkmcnt(13)\n"
    "ds_read_u16_d16_hi v153, v165 offset:33280\n"
    "ds_read_u16_d16_hi v150, v165 offset:1024\n"
    "s_waitcnt lgkmcnt(13)\n"
    "ds_read_u16_d16_hi v154, v165 offset:33792\n"
    "v_mfma_f32_32x32x16_bf16 v[132:147], a[24:27], a[148:151], v[132:147]\n"
    "ds_read_u16_d16_hi v151, v165 offset:1536\n"
    "s_waitcnt lgkmcnt(13)\n"
    "ds_read_u16_d16_hi v155, v165 offset:34304\n"
    "v_mul_f32 v9, v56, v156\n"
    "v_mul_f32 v56, v56, v160\n"
    "v_fma_f32 v9, -v72, v160, v9\n"
    "v_mfma_f32_32x32x16_bf16 v[116:131], a[56:59], a[148:151], v[116:131]\n"
    "v_fmac_f32 v56, v72, v156\n"
    "v_mul_f32 v10, v57, v157\n"
    "v_mul_f32 v57, v57, v161\n"
    "v_fma_f32 v10, -v73, v161, v10\n"
    "v_fmac_f32 v57, v73, v157\n"
    "s_waitcnt vmcnt(2)\n"
    "v_mfma_f32_32x32x16_bf16 v[84:99], a[12:15], a[152:155], v[84:99]\n"
    "v_cvt_pk_bf16_f32 v198, v9, v10\n"
    "v_cvt_pk_bf16_f32 v202, v56, v57\n"
    "v_mul_f32 v11, v58, v158\n"
    "v_mul_f32 v58, v58, v162\n"
    "v_fma_f32 v11, -v74, v162, v11\n"
    "v_mfma_f32_32x32x16_bf16 v[100:115], a[76:79], a[152:155], v[100:115]\n"
    "v_fmac_f32 v58, v74, v158\n"
    "v_mul_f32 v12, v59, v159\n"
    "v_mul_f32 v59, v59, v163\n"
    "v_fma_f32 v12, -v75, v163, v12\n"
    "v_fmac_f32 v59, v75, v159\n"
    "s_waitcnt vmcnt(0)\n"
    "v_mfma_f32_32x32x16_bf16 v[100:115], a[12:15], a[156:159], v[100:115]\n"
    "v_cvt_pk_bf16_f32 v199, v11, v12\n"
    "v_cvt_pk_bf16_f32 v203, v58, v59\n"
    "ds_read_u16_d16_hi v156, v165 offset:2048\n"
    "s_waitcnt lgkmcnt(13)\n"
    "ds_read_u16_d16_hi v160, v165 offset:34816\n"
    "ds_read_u16_d16_hi v157, v165 offset:2560\n"
    "v_mfma_f32_32x32x16_bf16 v[84:99], a[44:47], a[156:159], v[84:99]\n"
    "s_waitcnt lgkmcnt(13)\n"
    "ds_read_u16_d16_hi v161, v165 offset:35328\n"
    "ds_read_u16_d16_hi v158, v165 offset:3072\n"
    "s_waitcnt lgkmcnt(13)\n"
    "ds_read_u16_d16_hi v162, v165 offset:35840\n"
    "ds_read_u16_d16_hi v159, v165 offset:3584\n"
    "s_waitcnt lgkmcnt(13)\n"
    "ds_read_u16_d16_hi v163, v165 offset:36352\n"
    "v_mfma_f32_32x32x16_bf16 v[116:131], a[28:31], a[152:155], v[116:131]\n"
    "v_mul_f32 v9, v60, v166\n"
    "v_mul_f32 v60, v60, v170\n"
    "v_fma_f32 v9, -v76, v170, v9\n"
    "v_fmac_f32 v60, v76, v166\n"
    "v_mul_f32 v10, v61, v167\n"
    "v_mfma_f32_32x32x16_bf16 v[132:147], a[92:95], a[152:155], v[132:147]\n"
    "v_mul_f32 v61, v61, v171\n"
    "v_fma_f32 v10, -v77, v171, v10\n"
    "v_fmac_f32 v61, v77, v167\n"
    "v_cvt_pk_bf16_f32 v204, v9, v10\n"
    "v_cvt_pk_bf16_f32 v208, v60, v61\n"
    "v_mfma_f32_32x32x16_bf16 v[132:147], a[28:31], a[156:159], v[132:147]\n"
    "v_mul_f32 v11, v62, v168\n"
    "v_mul_f32 v62, v62, v172\n"
    "v_fma_f32 v11, -v78, v172, v11\n"
    "v_fmac_f32 v62, v78, v168\n"
    "v_mul_f32 v12, v63, v169\n"
    "v_mfma_f32_32x32x16_bf16 v[116:131], a[60:63], a[156:159], v[116:131]\n"
    "v_mul_f32 v63, v63, v173\n"
    "v_fma_f32 v12, -v79, v173, v12\n"
    "v_fmac_f32 v63, v79, v169\n"
    "v_cvt_pk_bf16_f32 v205, v11, v12\n"
    "v_cvt_pk_bf16_f32 v209, v62, v63\n"
    "v_mfma_f32_32x32x16_bf16 v[20:35], v[180:183], a[0:3], 0\n"
    "ds_read_u16_d16_hi v166, v165 offset:8192\n"
    "s_waitcnt lgkmcnt(13)\n"
    "ds_read_u16_d16_hi v170, v165 offset:40960\n"
    "ds_read_u16_d16_hi v167, v165 offset:8704\n"
    "s_waitcnt lgkmcnt(13)\n"
    "ds_read_u16_d16_hi v171, v165 offset:41472\n"
    "ds_read_u16_d16_hi v168, v165 offset:9216\n"
    "s_waitcnt lgkmcnt(13)\n"
    "ds_read_u16_d16_hi v172, v165 offset:41984\n"
    "v_mfma_f32_32x32x16_bf16 v[36:51], v[184:187], a[0:3], 0\n"
    "ds_read_u16_d16_hi v169, v165 offset:9728\n"
    "s_waitcnt lgkmcnt(13)\n"
    "ds_read_u16_d16_hi v173, v165 offset:42496\n"
    "v_mul_f32 v9, v64, v174\n"
    "v_mul_f32 v64, v64, v178\n"
    "v_fma_f32 v9, -v80, v178, v9\n"
    "v_mfma_f32_32x32x16_bf16 v[20:35], v[184:187], a[64:67], v[20:35]\n"
    "v_fmac_f32 v64, v80, v174\n"
    "v_mul_f32 v10, v65, v175\n"
    "v_mul_f32 v65, v65, v179\n"
    "v_fma_f32 v10, -v81, v179, v10\n"
    "v_fmac_f32 v65, v81, v175\n"
    "v_mfma_f32_32x32x16_bf16 v[36:51], v[180:183], a[32:35], v[36:51]\n"
    "v_cvt_pk_bf16_f32 v206, v9, v10\n"
    "v_cvt_pk_bf16_f32 v210, v64, v65\n"
    "v_mul_f32 v11, v66, v176\n"
    "v_mul_f32 v66, v66, v212\n"
    "v_fma_f32 v11, -v82, v212, v11\n"
    "v_mfma_f32_32x32x16_bf16 v[20:35], v[188:191], a[4:7], v[20:35]\n"
    "v_fmac_f32 v66, v82, v176\n"
    "v_mul_f32 v12, v67, v177\n"
    "v_mul_f32 v67, v67, v213\n"
    "v_fma_f32 v12, -v83, v213, v12\n"
    "v_fmac_f32 v67, v83, v177\n"
    "v_mfma_f32_32x32x16_bf16 v[36:51], v[192:195], a[4:7], v[36:51]\n"
    "v_cvt_pk_bf16_f32 v207, v11, v12\n"
    "v_cvt_pk_bf16_f32 v211, v66, v67\n"
    "ds_read_u16_d16_hi v174, v165 offset:10240\n"
    "s_waitcnt lgkmcnt(13)\n"
    "ds_read_u16_d16_hi v178, v165 offset:43008\n"
    "ds_read_u16_d16_hi v175, v165 offset:10752\n"
    "v_mfma_f32_32x32x16_bf16 v[20:35], v[192:195], a[68:71], v[20:35]\n"
    "s_waitcnt lgkmcnt(13)\n"
    "ds_read_u16_d16_hi v179, v165 offset:43520\n"
    "ds_read_u16_d16_hi v176, v165 offset:11264\n"
    "s_waitcnt lgkmcnt(13)\n"
    "ds_read_u16_d16_hi v212, v165 offset:44032\n"
    "ds_read_u16_d16_hi v177, v165 offset:11776\n"
    "s_waitcnt lgkmcnt(13)\n"
    "ds_read_u16_d16_hi v213, v165 offset:44544\n"
    "v_mfma_f32_32x32x16_bf16 v[36:51], v[188:191], a[36:39], v[36:51]\n"
    "v_mul_f32 v9, v84, v148\n"
    "v_mul_f32 v84, v84, v152\n"
    "v_fma_f32 v9, -v100, v152, v9\n"
    "v_fmac_f32 v84, v100, v148\n"
    "v_mul_f32 v10, v85, v149\n"
    "v_mfma_f32_32x32x16_bf16 v[20:35], v[196:199], a[8:11], v[20:35]\n"
    "v_mul_f32 v85, v85, v153\n"
    "v_fma_f32 v10, -v101, v153, v10\n"
    "v_fmac_f32 v85, v101, v149\n"
    "v_cvt_pk_bf16_f32 v216, v9, v10\n"
    "v_cvt_pk_bf16_f32 v220, v84, v85\n"
    "v_mfma_f32_32x32x16_bf16 v[36:51], v[200:203], a[8:11], v[36:51]\n"
    "v_mul_f32 v11, v86, v150\n"
    "v_mul_f32 v86, v86, v154\n"
    "v_fma_f32 v11, -v102, v154, v11\n"
    "v_fmac_f32 v86, v102, v150\n"
    "v_mul_f32 v12, v87, v151\n"
    "v_mfma_f32_32x32x16_bf16 v[20:35], v[200:203], a[72:75], v[20:35]\n"
    "v_mul_f32 v87, v87, v155\n"
    "v_fma_f32 v12, -v103, v155, v12\n"
    "v_fmac_f32 v87, v103, v151\n"
    "v_cvt_pk_bf16_f32 v217, v11, v12\n"
    "v_cvt_pk_bf16_f32 v221, v86, v87\n"
    "v_mfma_f32_32x32x16_bf16 v[36:51], v[196:199], a[40:43], v[36:51]\n"
    "ds_read_u16_d16_hi v148, v165 offset:16384\n"
    "s_waitcnt lgkmcnt(13)\n"
    "ds_read_u16_d16_hi v152, v165 offset:49152\n"
    "ds_read_u16_d16_hi v149, v165 offset:16896\n"
    "s_waitcnt lgkmcnt(13)\n"
    "ds_read_u16_d16_hi v153, v165 offset:49664\n"
    "ds_read_u16_d16_hi v150, v165 offset:17408\n"
    "s_waitcnt lgkmcnt(13)\n"
    "ds_read_u16_d16_hi v154, v165 offset:50176\n"
    "v_mfma_f32_32x32x16_bf16 v[20:35], v[204:207], a[12:15], v[20:35]\n"
    "ds_read_u16_d16_hi v151, v165 offset:17920\n"
    "s_waitcnt lgkmcnt(13)\n"
    "ds_read_u16_d16_hi v155, v165 offset:50688\n"
    "v_mul_f32 v9, v88, v156\n"
    "v_mul_f32 v88, v88, v160\n"
    "v_fma_f32 v9, -v104, v160, v9\n"
    "v_mfma_f32_32x32x16_bf16 v[36:51], v[208:211], a[12:15], v[36:51]\n"
    "v_fmac_f32 v88, v104, v156\n"
    "v_mul_f32 v10, v89, v157\n"
    "v_mul_f32 v89, v89, v161\n"
    "v_fma_f32 v10, -v105, v161, v10\n"
    "v_fmac_f32 v89, v105, v157\n"
    "v_mfma_f32_32x32x16_bf16 v[20:35], v[208:211], a[76:79], v[20:35]\n"
    "v_cvt_pk_bf16_f32 v218, v9, v10\n"
    "v_cvt_pk_bf16_f32 v222, v88, v89\n"
    "v_mul_f32 v11, v90, v158\n"
    "v_mul_f32 v90, v90, v162\n"
    "v_fma_f32 v11, -v106, v162, v11\n"
    "v_mfma_f32_32x32x16_bf16 v[36:51], v[204:207], a[44:47], v[36:51]\n"
    "v_fmac_f32 v90, v106, v158\n"
    "v_mul_f32 v12, v91, v159\n"
    "v_mul_f32 v91, v91, v163\n"
    "v_fma_f32 v12, -v107, v163, v12\n"
    "v_fmac_f32 v91, v107, v159\n"
    "v_mfma_f32_32x32x16_bf16 v[52:67], v[180:183], a[16:19], 0\n"
    "v_cvt_pk_bf16_f32 v219, v11, v12\n"
    "v_cvt_pk_bf16_f32 v223, v90, v91\n"
    "ds_read_u16_d16_hi v156, v165 offset:18432\n"
    "s_waitcnt lgkmcnt(13)\n"
    "ds_read_u16_d16_hi v160, v165 offset:51200\n"
    "ds_read_u16_d16_hi v157, v165 offset:18944\n"
    "v_mfma_f32_32x32x16_bf16 v[68:83], v[184:187], a[16:19], 0\n"
    "s_waitcnt lgkmcnt(13)\n"
    "ds_read_u16_d16_hi v161, v165 offset:51712\n"
    "ds_read_u16_d16_hi v158, v165 offset:19456\n"
    "s_waitcnt lgkmcnt(13)\n"
    "ds_read_u16_d16_hi v162, v165 offset:52224\n"
    "ds_read_u16_d16_hi v159, v165 offset:19968\n"
    "s_waitcnt lgkmcnt(13)\n"
    "ds_read_u16_d16_hi v163, v165 offset:52736\n"
    "v_mfma_f32_32x32x16_bf16 v[52:67], v[184:187], a[80:83], v[52:67]\n"
    "v_mul_f32 v9, v92, v166\n"
    "v_mul_f32 v92, v92, v170\n"
    "v_fma_f32 v9, -v108, v170, v9\n"
    "v_fmac_f32 v92, v108, v166\n"
    "v_mul_f32 v10, v93, v167\n"
    "v_mfma_f32_32x32x16_bf16 v[68:83], v[180:183], a[48:51], v[68:83]\n"
    "v_mul_f32 v93, v93, v171\n"
    "v_fma_f32 v10, -v109, v171, v10\n"
    "v_fmac_f32 v93, v109, v167\n"
    "v_cvt_pk_bf16_f32 v224, v9, v10\n"
    "v_cvt_pk_bf16_f32 v228, v92, v93\n"
    "v_mfma_f32_32x32x16_bf16 v[52:67], v[188:191], a[20:23], v[52:67]\n"
    "v_mul_f32 v11, v94, v168\n"
    "v_mul_f32 v94, v94, v172\n"
    "v_fma_f32 v11, -v110, v172, v11\n"
    "v_fmac_f32 v94, v110, v168\n"
    "v_mul_f32 v12, v95, v169\n"
    "v_mfma_f32_32x32x16_bf16 v[68:83], v[192:195], a[20:23], v[68:83]\n"
    "v_mul_f32 v95, v95, v173\n"
    "v_fma_f32 v12, -v111, v173, v12\n"
    "v_fmac_f32 v95, v111, v169\n"
    "v_cvt_pk_bf16_f32 v225, v11, v12\n"
    "v_cvt_pk_bf16_f32 v229, v94, v95\n"
    "v_mfma_f32_32x32x16_bf16 v[52:67], v[192:195], a[84:87], v[52:67]\n"
    "ds_read_u16_d16_hi v166, v165 offset:24576\n"
    "s_waitcnt lgkmcnt(13)\n"
    "ds_read_u16_d16_hi v170, v165 offset:57344\n"
    "ds_read_u16_d16_hi v167, v165 offset:25088\n"
    "s_waitcnt lgkmcnt(13)\n"
    "ds_read_u16_d16_hi v171, v165 offset:57856\n"
    "ds_read_u16_d16_hi v168, v165 offset:25600\n"
    "s_waitcnt lgkmcnt(13)\n"
    "ds_read_u16_d16_hi v172, v165 offset:58368\n"
    "v_mfma_f32_32x32x16_bf16 v[68:83], v[188:191], a[52:55], v[68:83]\n"
    "ds_read_u16_d16_hi v169, v165 offset:26112\n"
    "s_waitcnt lgkmcnt(13)\n"
    "ds_read_u16_d16_hi v173, v165 offset:58880\n"
    "v_mul_f32 v9, v96, v174\n"
    "v_mul_f32 v96, v96, v178\n"
    "v_fma_f32 v9, -v112, v178, v9\n"
    "v_mfma_f32_32x32x16_bf16 v[52:67], v[196:199], a[24:27], v[52:67]\n"
    "v_fmac_f32 v96, v112, v174\n"
    "v_mul_f32 v10, v97, v175\n"
    "v_mul_f32 v97, v97, v179\n"
    "v_fma_f32 v10, -v113, v179, v10\n"
    "v_fmac_f32 v97, v113, v175\n"
    "v_mfma_f32_32x32x16_bf16 v[68:83], v[200:203], a[24:27], v[68:83]\n"
    "v_cvt_pk_bf16_f32 v226, v9, v10\n"
    "v_cvt_pk_bf16_f32 v230, v96, v97\n"
    "v_mul_f32 v11, v98, v176\n"
    "v_mul_f32 v98, v98, v212\n"
    "v_fma_f32 v11, -v114, v212, v11\n"
    "v_mfma_f32_32x32x16_bf16 v[52:67], v[200:203], a[88:91], v[52:67]\n"
    "v_fmac_f32 v98, v114, v176\n"
    "v_mul_f32 v12, v99, v177\n"
    "v_mul_f32 v99, v99, v213\n"
    "v_fma_f32 v12, -v115, v213, v12\n"
    "v_fmac_f32 v99, v115, v177\n"
    "v_mfma_f32_32x32x16_bf16 v[68:83], v[196:199], a[56:59], v[68:83]\n"
    "v_cvt_pk_bf16_f32 v227, v11, v12\n"
    "v_cvt_pk_bf16_f32 v231, v98, v99\n"
    "ds_read_u16_d16_hi v174, v165 offset:26624\n"
    "s_waitcnt lgkmcnt(13)\n"
    "ds_read_u16_d16_hi v178, v165 offset:59392\n"
    "ds_read_u16_d16_hi v175, v165 offset:27136\n"
    "v_mfma_f32_32x32x16_bf16 v[52:67], v[204:207], a[28:31], v[52:67]\n"
    "s_waitcnt lgkmcnt(13)\n"
    "ds_read_u16_d16_hi v179, v165 offset:59904\n"
    "ds_read_u16_d16_hi v176, v165 offset:27648\n"
    "s_waitcnt lgkmcnt(13)\n"
    "ds_read_u16_d16_hi v212, v165 offset:60416\n"
    "ds_read_u16_d16_hi v177, v165 offset:28160\n"
    "s_waitcnt lgkmcnt(13)\n"
    "ds_read_u16_d16_hi v213, v165 offset:60928\n"
    "v_mfma_f32_32x32x16_bf16 v[68:83], v[208:211], a[28:31], v[68:83]\n"
    "v_mul_f32 v9, v116, v148\n"
    "v_mul_f32 v116, v116, v152\n"
    "v_fma_f32 v9, -v132, v152, v9\n"
    "v_fmac_f32 v116, v132, v148\n"
    "v_mul_f32 v10, v117, v149\n"
    "v_mfma_f32_32x32x16_bf16 v[52:67], v[208:211], a[92:95], v[52:67]\n"
    "v_mul_f32 v117, v117, v153\n"
    "v_fma_f32 v10, -v133, v153, v10\n"
    "v_fmac_f32 v117, v133, v149\n"
    "v_cvt_pk_bf16_f32 v232, v9, v10\n"
    "v_cvt_pk_bf16_f32 v236, v116, v117\n"
    "v_mfma_f32_32x32x16_bf16 v[68:83], v[204:207], a[60:63], v[68:83]\n"
    "v_mul_f32 v11, v118, v150\n"
    "v_mul_f32 v118, v118, v154\n"
    "v_fma_f32 v11, -v134, v154, v11\n"
    "v_fmac_f32 v118, v134, v150\n"
    "v_mul_f32 v12, v119, v151\n"
    "v_mfma_f32_32x32x16_bf16 v[84:99], v[216:219], a[0:3], 0\n"
    "v_mul_f32 v119, v119, v155\n"
    "v_fma_f32 v12, -v135, v155, v12\n"
    "v_fmac_f32 v119, v135, v151\n"
    "v_cvt_pk_bf16_f32 v233, v11, v12\n"
    "v_cvt_pk_bf16_f32 v237, v118, v119\n"
    "v_mfma_f32_32x32x16_bf16 v[100:115], v[220:223], a[0:3], 0\n"
    "v_mul_f32 v9, v120, v156\n"
    "v_mul_f32 v120, v120, v160\n"
    "v_fma_f32 v9, -v136, v160, v9\n"
    "v_fmac_f32 v120, v136, v156\n"
    "v_mul_f32 v10, v121, v157\n"
    "v_mfma_f32_32x32x16_bf16 v[84:99], v[220:223], a[64:67], v[84:99]\n"
    "v_mul_f32 v121, v121, v161\n"
    "v_fma_f32 v10, -v137, v161, v10\n"
    "v_fmac_f32 v121, v137, v157\n"
    "v_cvt_pk_bf16_f32 v234, v9, v10\n"
    "v_cvt_pk_bf16_f32 v238, v120, v121\n"
    "v_mfma_f32_32x32x16_bf16 v[100:115], v[216:219], a[32:35], v[100:115]\n"
    "v_mul_f32 v11, v122, v158\n"
    "v_mul_f32 v122, v122, v162\n"
    "v_fma_f32 v11, -v138, v162, v11\n"
    "v_fmac_f32 v122, v138, v158\n"
    "v_mul_f32 v12, v123, v159\n"
    "v_mfma_f32_32x32x16_bf16 v[84:99], v[224:227], a[4:7], v[84:99]\n"
    "v_mul_f32 v123, v123, v163\n"
    "v_fma_f32 v12, -v139, v163, v12\n"
    "v_fmac_f32 v123, v139, v159\n"
    "v_cvt_pk_bf16_f32 v235, v11, v12\n"
    "v_cvt_pk_bf16_f32 v239, v122, v123\n"
    "s_waitcnt lgkmcnt(8)\n"
    "v_mfma_f32_32x32x16_bf16 v[100:115], v[228:231], a[4:7], v[100:115]\n"
    "v_mul_f32 v9, v124, v166\n"
    "v_mul_f32 v124, v124, v170\n"
    "v_fma_f32 v9, -v140, v170, v9\n"
    "v_fmac_f32 v124, v140, v166\n"
    "v_mul_f32 v10, v125, v167\n"
    "v_mfma_f32_32x32x16_bf16 v[84:99], v[228:231], a[68:71], v[84:99]\n"
    "v_mul_f32 v125, v125, v171\n"
    "v_fma_f32 v10, -v141, v171, v10\n"
    "v_fmac_f32 v125, v141, v167\n"
    "v_cvt_pk_bf16_f32 v240, v9, v10\n"
    "v_cvt_pk_bf16_f32 v244, v124, v125\n"
    "v_mfma_f32_32x32x16_bf16 v[100:115], v[224:227], a[36:39], v[100:115]\n"
    "v_mul_f32 v11, v126, v168\n"
    "v_mul_f32 v126, v126, v172\n"
    "v_fma_f32 v11, -v142, v172, v11\n"
    "v_fmac_f32 v126, v142, v168\n"
    "v_mul_f32 v12, v127, v169\n"
    "v_mfma_f32_32x32x16_bf16 v[84:99], v[232:235], a[8:11], v[84:99]\n"
    "v_mul_f32 v127, v127, v173\n"
    "v_fma_f32 v12, -v143, v173, v12\n"
    "v_fmac_f32 v127, v143, v169\n"
    "v_cvt_pk_bf16_f32 v241, v11, v12\n"
    "v_cvt_pk_bf16_f32 v245, v126, v127\n"
    "s_waitcnt lgkmcnt(0)\n"
    "v_mfma_f32_32x32x16_bf16 v[100:115], v[236:239], a[8:11], v[100:115]\n"
    "v_mul_f32 v9, v128, v174\n"
    "v_mul_f32 v128, v128, v178\n"
    "v_fma_f32 v9, -v144, v178, v9\n"
    "v_fmac_f32 v128, v144, v174\n"
    "v_mul_f32 v10, v129, v175\n"
    "v_mfma_f32_32x32x16_bf16 v[84:99], v[236:239], a[72:75], v[84:99]\n"
    "v_mul_f32 v129, v129, v179\n"
    "v_fma_f32 v10, -v145, v179, v10\n"
    "v_fmac_f32 v129, v145, v175\n"
    "v_cvt_pk_bf16_f32 v242, v9, v10\n"
    "v_cvt_pk_bf16_f32 v246, v128, v129\n"
    "v_mfma_f32_32x32x16_bf16 v[100:115], v[232:235], a[40:43], v[100:115]\n"
    "v_mul_f32 v11, v130, v176\n"
    "v_mul_f32 v130, v130, v212\n"
    "v_fma_f32 v11, -v146, v212, v11\n"
    "v_fmac_f32 v130, v146, v176\n"
    "v_mul_f32 v12, v131, v177\n"
    "v_mul_f32 v131, v131, v213\n"
    "v_fma_f32 v12, -v147, v213, v12\n"
    "v_fmac_f32 v131, v147, v177\n"
    "v_cvt_pk_bf16_f32 v243, v11, v12\n"
    "v_cvt_pk_bf16_f32 v247, v130, v131\n"
    "s_nop 0\n"
    "v_mfma_f32_32x32x16_bf16 v[84:99], v[240:243], a[12:15], v[84:99]\n"
    "v_fmac_f32 v20, s72, v21\n"
    "v_fmac_f32 v36, s72, v37\n"
    "v_cvt_pk_bf16_f32 v20, v20, v36\n"
    "v_cvt_pk_bf16_f32 v21, v21, v37\n"
    "v_cvt_pk_bf16_f32 v22, v22, v38\n"
    "v_cvt_pk_bf16_f32 v23, v23, v39\n"
    "ds_write_b128 v6, v[20:23] offset:0\n"
    "v_cvt_pk_bf16_f32 v24, v24, v40\n"
    "v_cvt_pk_bf16_f32 v25, v25, v41\n"
    "v_cvt_pk_bf16_f32 v26, v26, v42\n"
    "v_cvt_pk_bf16_f32 v27, v27, v43\n"
    "v_xor_b32 v252, 32, v6\n"
    "ds_write_b128 v252, v[24:27] offset:0\n"
    "v_cvt_pk_bf16_f32 v28, v28, v44\n"
    "v_cvt_pk_bf16_f32 v29, v29, v45\n"
    "v_cvt_pk_bf16_f32 v30, v30, v46\n"
    "v_cvt_pk_bf16_f32 v31, v31, v47\n"
    "v_xor_b32 v252, 64, v6\n"
    "ds_write_b128 v252, v[28:31] offset:0\n"
    "v_cvt_pk_bf16_f32 v32, v32, v48\n"
    "v_cvt_pk_bf16_f32 v33, v33, v49\n"
    "v_cvt_pk_bf16_f32 v34, v34, v50\n"
    "v_cvt_pk_bf16_f32 v35, v35, v51\n"
    "v_xor_b32 v252, 96, v6\n"
    "ds_write_b128 v252, v[32:35] offset:0\n"
    "ds_read_b128 v[36:39], v7 offset:0\n"
    "ds_read_b128 v[40:43], v7 offset:1024\n"
    "ds_read_b128 v[44:47], v7 offset:2048\n"
    "ds_read_b128 v[48:51], v7 offset:3072\n"
    "v_mfma_f32_32x32x16_bf16 v[100:115], v[244:247], a[12:15], v[100:115]\n"
    "v_fmac_f32 v52, s72, v53\n"
    "v_fmac_f32 v68, s72, v69\n"
    "v_cvt_pk_bf16_f32 v52, v52, v68\n"
    "v_cvt_pk_bf16_f32 v53, v53, v69\n"
    "v_cvt_pk_bf16_f32 v54, v54, v70\n"
    "v_mfma_f32_32x32x16_bf16 v[84:99], v[244:247], a[76:79], v[84:99]\n"
    "v_cvt_pk_bf16_f32 v55, v55, v71\n"
    "ds_write_b128 v6, v[52:55] offset:4096\n"
    "v_cvt_pk_bf16_f32 v56, v56, v72\n"
    "v_mfma_f32_32x32x16_bf16 v[100:115], v[240:243], a[44:47], v[100:115]\n"
    "v_cvt_pk_bf16_f32 v57, v57, v73\n"
    "v_cvt_pk_bf16_f32 v58, v58, v74\n"
    "v_cvt_pk_bf16_f32 v59, v59, v75\n"
    "v_xor_b32 v252, 32, v6\n"
    "v_mfma_f32_32x32x16_bf16 v[116:131], v[216:219], a[16:19], 0\n"
    "ds_write_b128 v252, v[56:59] offset:4096\n"
    "v_cvt_pk_bf16_f32 v60, v60, v76\n"
    "v_mfma_f32_32x32x16_bf16 v[132:147], v[220:223], a[16:19], 0\n"
    "s_waitcnt lgkmcnt(5)\n"
    "s_mov_b32 s65, 0x0\n"
    "buffer_store_dwordx4 v[36:39], v8, s[52:55], s65 offen offset:128 sc1\n"
    "v_mfma_f32_32x32x16_bf16 v[116:131], v[220:223], a[80:83], v[116:131]\n"
    "s_waitcnt lgkmcnt(4)\n"
    "s_mov_b32 s65, 0x21400\n"
    "buffer_store_dwordx4 v[40:43], v8, s[52:55], s65 offen offset:128 sc1\n"
    "v_mfma_f32_32x32x16_bf16 v[132:147], v[216:219], a[48:51], v[132:147]\n"
    "s_waitcnt lgkmcnt(3)\n"
    "s_mov_b32 s65, 0x85000\n"
    "buffer_store_dwordx4 v[44:47], v8, s[52:55], s65 offen offset:128 sc1\n"
    "v_mfma_f32_32x32x16_bf16 v[116:131], v[224:227], a[20:23], v[116:131]\n"
    "s_waitcnt lgkmcnt(2)\n"
    "s_mov_b32 s65, 0xa6400\n"
    "buffer_store_dwordx4 v[48:51], v8, s[52:55], s65 offen offset:128 sc1\n"
    "v_mfma_f32_32x32x16_bf16 v[132:147], v[228:231], a[20:23], v[132:147]\n"
    "v_cvt_pk_bf16_f32 v61, v61, v77\n"
    "v_cvt_pk_bf16_f32 v62, v62, v78\n"
    "v_cvt_pk_bf16_f32 v63, v63, v79\n"
    "v_xor_b32 v252, 64, v6\n"
    "v_mfma_f32_32x32x16_bf16 v[116:131], v[228:231], a[84:87], v[116:131]\n"
    "ds_write_b128 v252, v[60:63] offset:4096\n"
    "v_cvt_pk_bf16_f32 v64, v64, v80\n"
    "v_cvt_pk_bf16_f32 v65, v65, v81\n"
    "v_mfma_f32_32x32x16_bf16 v[132:147], v[224:227], a[52:55], v[132:147]\n"
    "v_cvt_pk_bf16_f32 v66, v66, v82\n"
    "v_cvt_pk_bf16_f32 v67, v67, v83\n"
    "v_xor_b32 v252, 96, v6\n"
    "v_mfma_f32_32x32x16_bf16 v[116:131], v[232:235], a[24:27], v[116:131]\n"
    "ds_write_b128 v252, v[64:67] offset:4096\n"
    "ds_read_b128 v[68:71], v7 offset:4096\n"
    "v_mfma_f32_32x32x16_bf16 v[132:147], v[236:239], a[24:27], v[132:147]\n"
    "ds_read_b128 v[72:75], v7 offset:5120\n"
    "ds_read_b128 v[76:79], v7 offset:6144\n"
    "ds_read_b128 v[80:83], v7 offset:7168\n"
    "v_cvt_pk_bf16_f32 v84, v84, v100\n"
    "v_mfma_f32_32x32x16_bf16 v[116:131], v[236:239], a[88:91], v[116:131]\n"
    "v_cvt_pk_bf16_f32 v85, v85, v101\n"
    "v_cvt_pk_bf16_f32 v86, v86, v102\n"
    "v_cvt_pk_bf16_f32 v87, v87, v103\n"
    "v_mfma_f32_32x32x16_bf16 v[132:147], v[232:235], a[56:59], v[132:147]\n"
    "ds_write_b128 v6, v[84:87] offset:0\n"
    "v_cvt_pk_bf16_f32 v88, v88, v104\n"
    "v_cvt_pk_bf16_f32 v89, v89, v105\n"
    "v_mfma_f32_32x32x16_bf16 v[116:131], v[240:243], a[28:31], v[116:131]\n"
    "v_cvt_pk_bf16_f32 v90, v90, v106\n"
    "v_cvt_pk_bf16_f32 v91, v91, v107\n"
    "v_xor_b32 v252, 32, v6\n"
    "v_mfma_f32_32x32x16_bf16 v[132:147], v[244:247], a[28:31], v[132:147]\n"
    "s_waitcnt lgkmcnt(4)\n"
    "s_mov_b32 s65, 0x10a000\n"
    "buffer_store_dwordx4 v[68:71], v8, s[52:55], s65 offen offset:128 sc1\n"
    "v_mfma_f32_32x32x16_bf16 v[116:131], v[244:247], a[92:95], v[116:131]\n"
    "s_waitcnt lgkmcnt(3)\n"
    "s_mov_b32 s65, 0x12b400\n"
    "buffer_store_dwordx4 v[72:75], v8, s[52:55], s65 offen offset:128 sc1\n"
    "v_mfma_f32_32x32x16_bf16 v[132:147], v[240:243], a[60:63], v[132:147]\n"
    "s_waitcnt lgkmcnt(2)\n"
    "s_mov_b32 s65, 0x18f000\n"
    "buffer_store_dwordx4 v[76:79], v8, s[52:55], s65 offen offset:128 sc1\n"
    "ds_write_b128 v252, v[88:91] offset:0\n"
    "s_waitcnt lgkmcnt(2)\n"
    "s_mov_b32 s65, 0x1b0400\n"
    "buffer_store_dwordx4 v[80:83], v8, s[52:55], s65 offen offset:128 sc1\n"
    "v_cvt_pk_bf16_f32 v92, v92, v108\n"
    "v_cvt_pk_bf16_f32 v93, v93, v109\n"
    "v_cvt_pk_bf16_f32 v94, v94, v110\n"
    "v_cvt_pk_bf16_f32 v95, v95, v111\n"
    "v_xor_b32 v252, 64, v6\n"
    "ds_write_b128 v252, v[92:95] offset:0\n"
    "v_cvt_pk_bf16_f32 v96, v96, v112\n"
    "v_cvt_pk_bf16_f32 v97, v97, v113\n"
    "v_cvt_pk_bf16_f32 v98, v98, v114\n"
    "v_cvt_pk_bf16_f32 v99, v99, v115\n"
    "v_xor_b32 v252, 96, v6\n"
    "ds_write_b128 v252, v[96:99] offset:0\n"
    "ds_read_b128 v[100:103], v7 offset:0\n"
    "ds_read_b128 v[104:107], v7 offset:1024\n"
    "ds_read_b128 v[108:111], v7 offset:2048\n"
    "ds_read_b128 v[112:115], v7 offset:3072\n"
    "s_nop 3\n"
    "v_cvt_pk_bf16_f32 v116, v116, v132\n"
    "v_cvt_pk_bf16_f32 v117, v117, v133\n"
    "v_cvt_pk_bf16_f32 v118, v118, v134\n"
    "v_cvt_pk_bf16_f32 v119, v119, v135\n"
    "ds_write_b128 v6, v[116:119] offset:4096\n"
    "v_cvt_pk_bf16_f32 v120, v120, v136\n"
    "v_cvt_pk_bf16_f32 v121, v121, v137\n"
    "v_cvt_pk_bf16_f32 v122, v122, v138\n"
    "v_cvt_pk_bf16_f32 v123, v123, v139\n"
    "v_xor_b32 v252, 32, v6\n"
    "s_waitcnt lgkmcnt(4)\n"
    "s_mov_b32 s65, 0x0\n"
    "buffer_store_dwordx4 v[100:103], v8, s[52:55], s65 offen offset:0 sc1\n"
    "ds_write_b128 v252, v[120:123] offset:4096\n"
    "s_waitcnt lgkmcnt(4)\n"
    "s_mov_b32 s65, 0x21400\n"
    "buffer_store_dwordx4 v[104:107], v8, s[52:55], s65 offen offset:0 sc1\n"
    "v_cvt_pk_bf16_f32 v124, v124, v140\n"
    "s_waitcnt lgkmcnt(3)\n"
    "s_mov_b32 s65, 0x85000\n"
    "buffer_store_dwordx4 v[108:111], v8, s[52:55], s65 offen offset:0 sc1\n"
    "v_cvt_pk_bf16_f32 v125, v125, v141\n"
    "s_waitcnt lgkmcnt(2)\n"
    "s_mov_b32 s65, 0xa6400\n"
    "buffer_store_dwordx4 v[112:115], v8, s[52:55], s65 offen offset:0 sc1\n"
    "v_cvt_pk_bf16_f32 v126, v126, v142\n"
    "v_cvt_pk_bf16_f32 v127, v127, v143\n"
    "v_xor_b32 v252, 64, v6\n"
    "ds_write_b128 v252, v[124:127] offset:4096\n"
    "v_cvt_pk_bf16_f32 v128, v128, v144\n"
    "v_cvt_pk_bf16_f32 v129, v129, v145\n"
    "v_cvt_pk_bf16_f32 v130, v130, v146\n"
    "v_cvt_pk_bf16_f32 v131, v131, v147\n"
    "v_xor_b32 v252, 96, v6\n"
    "ds_write_b128 v252, v[128:131] offset:4096\n"
    "ds_read_b128 v[132:135], v7 offset:4096\n"
    "ds_read_b128 v[136:139], v7 offset:5120\n"
    "ds_read_b128 v[140:143], v7 offset:6144\n"
    "ds_read_b128 v[144:147], v7 offset:7168\n"
    "s_waitcnt lgkmcnt(3)\n"
    "s_mov_b32 s65, 0x10a000\n"
    "buffer_store_dwordx4 v[132:135], v8, s[52:55], s65 offen offset:0 sc1\n"
    "s_waitcnt lgkmcnt(2)\n"
    "s_mov_b32 s65, 0x12b400\n"
    "buffer_store_dwordx4 v[136:139], v8, s[52:55], s65 offen offset:0 sc1\n"
    "s_waitcnt lgkmcnt(1)\n"
    "s_mov_b32 s65, 0x18f000\n"
    "buffer_store_dwordx4 v[140:143], v8, s[52:55], s65 offen offset:0 sc1\n"
    "s_waitcnt lgkmcnt(0)\n"
    "s_mov_b32 s65, 0x1b0400\n"
    "buffer_store_dwordx4 v[144:147], v8, s[52:55], s65 offen offset:0 sc1\n"
    "v_cmp_eq_u32 vcc, 0, v0\n"
    "s_lshl_b32 s62, s61, 2\n"
    "s_add_u32 s62, s62, s60\n"
    "s_lshl_b32 s62, s62, 7\n"
    "s_add_u32 s62, s62, 4096\n"
    "v_mov_b32 v9, 0x7ffffff0\n"
    "v_mov_b32 v10, s62\n"
    "v_cndmask_b32 v9, v9, v10, vcc\n"
    "s_waitcnt vmcnt(0)\n"
    "buffer_store_dwordx2 v[248:249], v9, s[52:55], 0 offen offset:0 sc1\n"
    ".Lp_end:\n"
      :: "s"(ka), "s"(wsel), "s"(0), "s"(0), "s"(0), "s"(w), "s"(xcd * 4 + idx), "s"(b), "s"((unsigned)(size_t)smem)
      : "v0", "v1", "v2", "v3", "v4", "v5", "v6", "v7", "v8", "v9", "v10", "v11", "v12", "v13", "v14", "v15", "v16", "v17", "v18", "v19", "v20", "v21", "v22", "v23", "v24", "v25", "v26", "v27", "v28", "v29", "v30", "v31", "v32", "v33", "v34", "v35", "v36", "v37", "v38", "v39", "v40", "v41", "v42", "v43", "v44", "v45", "v46", "v47", "v48", "v49", "v50", "v51", "v52", "v53", "v54", "v55", "v56", "v57", "v58", "v59", "v60", "v61", "v62", "v63", "v64", "v65", "v66", "v67", "v68", "v69", "v70", "v71", "v72", "v73", "v74", "v75", "v76", "v77", "v78", "v79", "v80", "v81", "v82", "v83", "v84", "v85", "v86", "v87", "v88", "v89", "v90", "v91", "v92", "v93", "v94", "v95", "v96", "v97", "v98", "v99", "v100", "v101", "v102", "v103", "v104", "v105", "v106", "v107", "v108", "v109", "v110", "v111", "v112", "v113", "v114", "v115", "v116", "v117", "v118", "v119", "v120", "v121", "v122", "v123", "v124", "v125", "v126", "v127", "v128", "v129", "v130", "v131", "v132", "v133", "v134", "v135", "v136", "v137", "v138", "v139", "v140", "v141", "v142", "v143", "v144", "v145", "v146", "v147", "v148", "v149", "v150", "v151", "v152", "v153", "v154", "v155", "v156", "v157", "v158", "v159", "v160", "v161", "v162", "v163", "v164", "v165", "v166", "v167", "v168", "v169", "v170", "v171", "v172", "v173", "v174", "v175", "v176", "v177", "v178", "v179", "v180", "v181", "v182", "v183", "v184", "v185", "v186", "v187", "v188", "v189", "v190", "v191", "v192", "v193", "v194", "v195", "v196", "v197", "v198", "v199", "v200", "v201", "v202", "v203", "v204", "v205", "v206", "v207", "v208", "v209", "v210", "v211", "v212", "v213", "v214", "v215", "v216", "v217", "v218", "v219", "v220", "v221", "v222", "v223", "v224", "v225", "v226", "v227", "v228", "v229", "v230", "v231", "v232", "v233", "v234", "v235", "v236", "v237", "v238", "v239", "v240", "v241", "v242", "v243", "v244", "v245", "v246", "v247", "v248", "v249", "v250", "v251", "v252", "v253", "v254", "v255", "a0", "a1", "a2", "a3", "a4", "a5", "a6", "a7", "a8", "a9", "a10", "a11", "a12", "a13", "a14", "a15", "a16", "a17", "a18", "a19", "a20", "a21", "a22", "a23", "a24", "a25", "a26", "a27", "a28", "a29", "a30", "a31", "a32", "a33", "a34", "a35", "a36", "a37", "a38", "a39", "a40", "a41", "a42", "a43", "a44", "a45", "a46", "a47", "a48", "a49", "a50", "a51", "a52", "a53", "a54", "a55", "a56", "a57", "a58", "a59", "a60", "a61", "a62", "a63", "a64", "a65", "a66", "a67", "a68", "a69", "a70", "a71", "a72", "a73", "a74", "a75", "a76", "a77", "a78", "a79", "a80", "a81", "a82", "a83", "a84", "a85", "a86", "a87", "a88", "a89", "a90", "a91", "a92", "a93", "a94", "a95", "a96", "a97", "a98", "a99", "a100", "a101", "a102", "a103", "a104", "a105", "a106", "a107", "a108", "a109", "a110", "a111", "a112", "a113", "a114", "a115", "a116", "a117", "a118", "a119", "a120", "a121", "a122", "a123", "a124", "a125", "a126", "a127", "a128", "a129", "a130", "a131", "a132", "a133", "a134", "a135", "a136", "a137", "a138", "a139", "a140", "a141", "a142", "a143", "a144", "a145", "a146", "a147", "a148", "a149", "a150", "a151", "a152", "a153", "a154", "a155", "a156", "a157", "a158", "a159", "a160", "a161", "a162", "a163", "a164", "a165", "a166", "a167", "a168", "a169", "a170", "a171", "a172", "a173", "a174", "a175", "a176", "a177", "a178", "a179", "a180", "a181", "a182", "a183", "a184", "a185", "a186", "a187", "a188", "a189", "a190", "a191", "a192", "a193", "a194", "a195", "a196", "a197", "a198", "a199", "a200", "a201", "a202", "a203", "a204", "a205", "a206", "a207", "a208", "a209", "a210", "a211", "a212", "a213", "a214", "a215", "a216", "a217", "a218", "a219", "a220", "a221", "a222", "a223", "a224", "a225", "a226", "a227", "a228", "a229", "a230", "a231", "a232", "a233", "a234", "a235", "a236", "a237", "a238", "a239", "a240", "a241", "a242", "a243", "a244", "a245", "a246", "a247", "a248", "a249", "a250", "a251", "a252", "a253", "a254", "a255", "s30", "s31", "s33", "s34", "s35", "s36", "s37", "s38", "s39", "s40", "s41", "s42", "s43", "s44", "s45", "s46", "s47", "s48", "s49", "s50", "s51", "s52", "s53", "s54", "s55", "s56", "s57", "s58", "s59", "s60", "s61", "s62", "s63", "s64", "s65", "s66", "s67", "s68", "s69", "s70", "s71", "s72", "s73", "s74", "s75", "s76", "s77", "s78", "s79", "s80", "s81", "s82", "s83", "s84", "s85", "s86", "s87", "s88", "s89", "s90", "s91", "s92", "s93", "s94", "s95", "s96", "s97", "s98", "s99", "vcc", "scc", "memory");
    return;
  }
  const int n = idx - 4, n7 = (n * 37) >> 8, r7 = n - 7 * n7;
  const int rt = (xcd >> 1) * 4 + n7, ct = (xcd & 1) * 7 + r7, kh = xcd & 1, cb = r7, blk = rt * 14 + ct;
  asm volatile(
    ".Lm_begin:\n"
    "s_load_dwordx2 s[36:37], %0, 0\n"
    "s_load_dwordx2 s[48:49], %0, 56\n"
    "s_load_dwordx2 s[40:41], %0, %1\n"
    "s_load_dwordx2 s[44:45], %0, 40\n"
    "s_load_dwordx2 s[52:53], %0, 48\n"
    "v_mbcnt_lo_u32_b32 v0, -1, 0\n"
    "v_mbcnt_hi_u32_b32 v0, -1, v0\n"
    "s_mov_b32 s67, %5\n"
    "s_getpc_b64 s[76:77]\n"
    ".Lm_pc:\n"
    "s_sub_u32 s76, s76, .Lm_pc-.Lm_begin\n"
    "s_subb_u32 s77, s77, 0\n"
    "s_and_b32 s77, s77, 0xffff\n"
    "s_mov_b32 s78, .Lp_end-.Lm_begin\n"
    "s_mov_b32 s79, 0x20000\n"
    "s_lshr_b32 s59, %11, 3\n"
    "s_xor_b32 s59, s59, 4\n"
    "s_lshl_b32 s59, s59, 2\n"
    "s_add_u32 s59, s59, s67\n"
    "s_lshl_b32 s59, s59, 10\n"
    "v_lshlrev_b32 v240, 4, v0\n"
    "buffer_load_dwordx4 v[244:247], v240, s[76:79], s59 offen offset:0\n"
    "s_lshl_b32 s74, %7, 2\n"
    "s_add_u32 s74, s74, s67\n"
    "s_lshl_b32 s74, s74, 1\n"
    "s_bfe_u32 s73, s74, 0x50000\n"
    "s_lshl_b32 s73, s73, 4\n"
    "s_add_u32 s73, s73, %6\n"
    "s_lshl_b32 s73, s73, 2\n"
    "s_lshr_b32 s74, s74, 5\n"
    "s_add_u32 s73, s73, s74\n"
    "s_mul_i32 s73, s73, 896\n"
    "s_add_u32 s74, s73, 57344\n"
    "v_cmp_gt_u32 vcc, 56, v0\n"
    "v_lshlrev_b32 v190, 4, v0\n"
    "v_mov_b32 v192, 0x7ffffff0\n"
    "v_cndmask_b32 v190, v192, v190, vcc\n"
    "v_lshrrev_b32 v176, 3, v0\n"
    "v_lshrrev_b32 v177, 4, v0\n"
    "v_and_b32 v178, 7, v0\n"
    "s_lshl_b32 s59, %6, 2\n"
    "s_add_u32 s59, s59, s67\n"
    "s_mul_i32 s59, s59, 896\n"
    "s_lshl_b32 s56, s67, 12\n"
    "v_mul_u32_u24 v179, 57344, v176\n"
    "v_xor_b32 v180, v178, v177\n"
    "v_lshlrev_b32 v180, 4, v180\n"
    "v_add3_u32 v1, v179, v180, s59\n"
    "v_xor_b32 v180, 64, v1\n"
    "v_add_u32 v2, 457728, v180\n"
    "v_add_u32 v3, 915456, v1\n"
    "v_add_u32 v4, 1373184, v180\n"
    "s_waitcnt lgkmcnt(0)\n"
    "s_and_b32 s37, s37, 0xffff\n"
    "s_mov_b32 s38, 0x1c0000\n"
    "s_mov_b32 s39, 0x20000\n"
    "buffer_load_dwordx4 v[192:195], v190, s[36:39], s73 offen offset:0\n"
    "buffer_load_dwordx4 v[196:199], v190, s[36:39], s74 offen offset:0\n"
    "s_add_u32 m0, s56, 0x0\n"
    "s_mov_b32 s57, 0x0\n"
    "s_nop 0\n"
    "buffer_load_dwordx4 v1, s[36:39], s57 offen offset:0 lds\n"
    "buffer_load_dwordx4 v2, s[36:39], s57 offen offset:1024 lds\n"
    "buffer_load_dwordx4 v3, s[36:39], s57 offen offset:2048 lds\n"
    "buffer_load_dwordx4 v4, s[36:39], s57 offen offset:3072 lds\n"
    "s_and_b32 s49, s49, 0xffff\n"
    "s_mov_b32 s50, 0x1600000\n"
    "s_mov_b32 s51, 0x20000\n"
    "v_mov_b32 v186, 0\n"
    "buffer_load_dwordx2 v[46:47], v186, s[48:51], 0 offen offset:0 sc1\n"
    "s_mov_b32 s69, %7\n"
    "s_and_b32 s41, s41, 0xffff\n"
    "s_mov_b32 s42, 0x188000\n"
    "s_mov_b32 s43, 0x20000\n"
    "s_and_b32 s59, s67, 1\n"
    "s_mul_i32 s59, s59, 114688\n"
    "s_lshl_b32 s60, s69, 8\n"
    "s_add_u32 s59, s59, s60\n"
    "v_mul_u32_u24 v179, 3584, v176\n"
    "v_and_b32 v180, 1, v177\n"
    "v_lshlrev_b32 v180, 2, v180\n"
    "v_xor_b32 v180, v180, v178\n"
    "v_lshlrev_b32 v181, 4, v180\n"
    "v_add3_u32 v5, v179, v181, s59\n"
    "v_add_u32 v6, 27648, v5\n"
    "v_add_u32 v7, 55296, v5\n"
    "v_add_u32 v8, 82944, v5\n"
    "s_mov_b32 s68, %6\n"
    "s_mov_b32 s69, %7\n"
    "v_and_b32 v20, 31, v0\n"
    "v_lshrrev_b32 v21, 5, v0\n"
    "s_lshl_b32 s59, s69, 4\n"
    "v_and_b32 v179, 15, v20\n"
    "v_add_u32 v179, s59, v179\n"
    "v_lshrrev_b32 v180, 4, v20\n"
    "v_cvt_f32_u32 v180, v180\n"
    "v_mul_f32 v188, 0x3e800000, v180\n"
    "v_lshl_add_u32 v180, s69, 5, v20\n"
    "v_cmp_eq_u32 vcc, 16, v180\n"
    "v_mov_b32 v189, 224\n"
    "s_lshl_b32 s59, s67, 1\n"
    "s_nop 0\n"
    "v_cndmask_b32 v179, v179, v189, vcc\n"
    "v_cndmask_b32 v188, v188, 0, vcc\n"
    "v_cvt_f32_u32 v179, v179\n"
    "v_add_u32 v180, s59, v21\n"
    "v_cvt_f32_u32 v180, v180\n"
    "v_mul_f32 v187, 0x3b124925, v179\n"
    "v_mul_f32 v180, 0x41000000, v180\n"
    "v_mul_f32 v22, 0x3e124925, v179\n"
    "v_fmac_f32 v188, v187, v180\n"
    "s_lshl_b32 s59, s67, 10\n"
    "s_add_u32 s59, s59, 147456\n"
    "v_lshl_add_u32 v239, v0, 4, s59\n"
    "v_fmamk_f32 v24, v187, 0x00000000, v188\n"
    "v_fmamk_f32 v25, v187, 0x3f800000, v188\n"
    "v_fmamk_f32 v26, v187, 0x40000000, v188\n"
    "v_fmamk_f32 v27, v187, 0x40400000, v188\n"
    "v_fmamk_f32 v28, v187, 0x40800000, v188\n"
    "v_fmamk_f32 v29, v187, 0x40a00000, v188\n"
    "v_fmamk_f32 v30, v187, 0x40c00000, v188\n"
    "v_fmamk_f32 v31, v187, 0x40e00000, v188\n"
    "v_cos_f32 v24, v24\n"
    "v_cos_f32 v25, v25\n"
    "v_cos_f32 v26, v26\n"
    "v_cos_f32 v27, v27\n"
    "v_cos_f32 v28, v28\n"
    "v_cos_f32 v29, v29\n"
    "v_cos_f32 v30, v30\n"
    "v_cos_f32 v31, v31\n"
    "v_cvt_pk_bf16_f32 v40, v24, v25\n"
    "v_cvt_pk_bf16_f32 v41, v26, v27\n"
    "v_cvt_pk_bf16_f32 v42, v28, v29\n"
    "v_cvt_pk_bf16_f32 v43, v30, v31\n"
    "ds_write_b128 v239, v[40:43] offset:0\n"
    "v_lshlrev_b32 v44, 4, v0\n"
    "v_add_u32 v44, 147456, v44\n"
    "v_bfe_u32 v179, v20, 1, 3\n"
    "v_xor_b32 v179, v179, v21\n"
    "v_lshlrev_b32 v179, 4, v179\n"
    "v_lshlrev_b32 v180, 7, v20\n"
    "v_add3_u32 v9, v179, v180, s56\n"
    "v_xor_b32 v10, 32, v9\n"
    "v_xor_b32 v11, 64, v9\n"
    "v_xor_b32 v12, 96, v9\n"
    "v_xor_b32 v13, 4096, v9\n"
    "v_xor_b32 v14, 4096, v10\n"
    "v_xor_b32 v15, 4096, v11\n"
    "v_xor_b32 v16, 4096, v12\n"
    "s_and_b32 s60, s67, 1\n"
    "s_lshl_b32 s60, s60, 6\n"
    "v_bfe_u32 v179, v0, 2, 2\n"
    "v_lshl_add_u32 v179, v21, 3, v179\n"
    "v_lshlrev_b32 v179, 7, v179\n"
    "v_bfe_u32 v180, v0, 3, 1\n"
    "v_lshlrev_b32 v180, 6, v180\n"
    "v_xor_b32 v180, s60, v180\n"
    "v_add_u32 v179, v179, v180\n"
    "v_bfe_u32 v180, v0, 4, 1\n"
    "v_lshl_add_u32 v179, v180, 5, v179\n"
    "v_and_b32 v180, 3, v0\n"
    "v_lshl_add_u32 v17, v180, 3, v179\n"
    "v_add_u32 v18, 65536, v17\n"
    "v_add_u32 v19, 131072, v17\n"
    "v_mov_b32 v200, 0\n"
    "v_mov_b32 v201, 0\n"
    "v_mov_b32 v202, 0\n"
    "v_mov_b32 v203, 0\n"
    "s_mov_b32 s60, 0x3f803f80\n"
    "s_lshl_b32 s59, s68, 4\n"
    "s_add_u32 s59, s59, s69\n"
    "s_lshl_b32 s59, s59, 2\n"
    "s_add_u32 s59, s59, s67\n"
    "s_lshl_b32 s59, s59, 5\n"
    "s_add_u32 s59, s59, 135168\n"
    "v_lshrrev_b32 v190, 1, v0\n"
    "v_add_u32 v190, s59, v190\n"
    "v_and_b32 v204, 31, v0\n"
    "v_cmp_eq_u32 vcc, 0, v204\n"
    "v_mov_b32 v204, 0x7ffffff0\n"
    "s_add_u32 m0, s56, 0x10000\n"
    "s_mov_b32 s58, 0x0\n"
    "s_nop 0\n"
    "buffer_load_dwordx4 v5, s[40:43], s58 offen offset:0 lds\n"
    "buffer_load_dwordx4 v6, s[40:43], s58 offen offset:1024 lds\n"
    "s_waitcnt vmcnt(7)\n"
    "buffer_load_dwordx4 v7, s[40:43], s58 offen offset:2048 lds\n"
    "v_cndmask_b32 v190, v204, v190, vcc\n"
    "v_dot2c_f32_bf16 v200, s60, v192\n"
    "v_dot2c_f32_bf16 v202, v192, v192\n"
    "v_dot2c_f32_bf16 v201, s60, v196\n"
    "v_dot2c_f32_bf16 v203, v196, v196\n"
    "v_dot2c_f32_bf16 v200, s60, v193\n"
    "v_dot2c_f32_bf16 v202, v193, v193\n"
    "v_dot2c_f32_bf16 v201, s60, v197\n"
    "v_dot2c_f32_bf16 v203, v197, v197\n"
    "v_dot2c_f32_bf16 v200, s60, v194\n"
    "v_dot2c_f32_bf16 v202, v194, v194\n"
    "v_dot2c_f32_bf16 v201, s60, v198\n"
    "v_dot2c_f32_bf16 v203, v198, v198\n"
    "v_dot2c_f32_bf16 v200, s60, v195\n"
    "v_dot2c_f32_bf16 v202, v195, v195\n"
    "v_dot2c_f32_bf16 v201, s60, v199\n"
    "v_dot2c_f32_bf16 v203, v199, v199\n"
    "buffer_load_dwordx4 v8, s[40:43], s58 offen offset:3072 lds\n"
    "s_add_u32 m0, s56, 0x4000\n"
    "s_mov_b32 s57, 0x80\n"
    "s_nop 0\n"
    "buffer_load_dwordx4 v1, s[36:39], s57 offen offset:0 lds\n"
    "buffer_load_dwordx4 v2, s[36:39], s57 offen offset:1024 lds\n"
    "s_waitcnt vmcnt(6)\n"
    "buffer_load_dwordx4 v3, s[36:39], s57 offen offset:2048 lds\n"
    "v_readfirstlane_b32 s62, v46\n"
    "buffer_load_dwordx4 v4, s[36:39], s57 offen offset:3072 lds\n"
    "v_readfirstlane_b32 s63, v47\n"
    "v_permlane32_swap_b32 v200, v201\n"
    "v_permlane32_swap_b32 v202, v203\n"
    "v_add_f32 v200, v200, v201\n"
    "v_add_f32 v202, v202, v203\n"
    "s_mul_i32 s62, s62, 1664525\n"
    "s_mul_i32 s63, s63, 22695477\n"
    "s_nop 1\n"
    "v_permlane16_swap_b32 v200, v202\n"
    "v_add_f32 v200, v200, v202\n"
    "s_add_u32 s62, s62, 1013904223\n"
    "s_add_u32 s63, s63, 0x9e3779b9\n"
    "s_nop 1\n"
    "v_add_f32_dpp v200, v200, v200 row_ror:8 row_mask:0xf bank_mask:0xf\n"
    "v_mov_b32 v46, s62\n"
    "v_mov_b32 v47, s63\n"
    "v_add_f32_dpp v200, v200, v200 row_ror:4 row_mask:0xf bank_mask:0xf\n"
    "v_mov_b32 v206, s62\n"
    "v_mov_b32 v207, s63\n"
    "v_add_f32_dpp v200, v200, v200 row_ror:2 row_mask:0xf bank_mask:0xf\n"
    "s_nop 1\n"
    "v_add_f32_dpp v200, v200, v200 row_ror:1 row_mask:0xf bank_mask:0xf\n"
    "v_mov_b32 v202, v200\n"
    "s_nop 1\n"
    "v_permlane16_swap_b32 v200, v202\n"
    "v_mul_f32 v200, 0x3b124925, v200\n"
    "v_mul_f32 v202, 0x3b124925, v202\n"
    "v_fma_f32 v202, -v200, v200, v202\n"
    "v_max_f32 v202, 0, v202\n"
    "v_cvt_pk_bf16_f32 v202, v202, v202\n"
    "v_lshlrev_b32 v202, 16, v202\n"
    "v_add_f32 v202, 0x3727c5ac, v202\n"
    "v_cvt_pk_bf16_f32 v202, v202, v202\n"
    "v_lshlrev_b32 v202, 16, v202\n"
    "v_sqrt_f32 v202, v202\n"
    "s_nop 0\n"
    "v_cvt_pk_bf16_f32 v204, v200, v202\n"
    "v_and_b32 v202, 0xffff0000, v204\n"
    "s_add_u32 m0, s56, 0x8000\n"
    "s_mov_b32 s57, 0x100\n"
    "s_nop 0\n"
    "buffer_load_dwordx4 v1, s[36:39], s57 offen offset:0 lds\n"
    "v_rcp_f32 v205, v202\n"
    "buffer_store_dwordx4 v[204:207], v190, s[48:51], 0 offen offset:0 sc1\n"
    "buffer_load_dwordx4 v2, s[36:39], s57 offen offset:1024 lds\n"
    "buffer_load_dwordx4 v3, s[36:39], s57 offen offset:2048 lds\n"
    "s_waitcnt vmcnt(8)\n"
    "buffer_load_dwordx4 v4, s[36:39], s57 offen offset:3072 lds\n"
    "s_waitcnt lgkmcnt(0)\n"
    "s_barrier\n"
    "ds_read_b64_tr_b16 a[224:225], v18 offset:0\n"
    "ds_read_b64_tr_b16 a[226:227], v18 offset:512\n"
    "ds_read_b64_tr_b16 a[228:229], v18 offset:8192\n"
    "ds_read_b64_tr_b16 a[230:231], v18 offset:8704\n"
    "ds_read_b128 a[0:3], v9 offset:0\n"
    "ds_read_b128 v[48:51], v44 offset:0\n"
    "ds_read_b128 a[112:115], v13 offset:0\n"
    "ds_read_b64_tr_b16 a[232:233], v18 offset:2048\n"
    "ds_read_b64_tr_b16 a[234:235], v18 offset:2560\n"
    "ds_read_b64_tr_b16 a[236:237], v18 offset:10240\n"
    "ds_read_b64_tr_b16 a[238:239], v18 offset:10752\n"
    "ds_read_b128 a[4:7], v10 offset:0\n"
    "ds_read_b128 v[52:55], v44 offset:1024\n"
    "ds_read_b128 a[116:119], v14 offset:0\n"
    "s_add_u32 m0, s56, 0x14000\n"
    "s_mov_b32 s58, 0x38000\n"
    "s_nop 0\n"
    "buffer_load_dwordx4 v5, s[40:43], s58 offen offset:0 lds\n"
    "s_mov_b32 s70, %8\n"
    "s_mov_b32 s71, %9\n"
    "s_mov_b32 s72, %10\n"
    "s_and_b32 s45, s45, 0xffff\n"
    "s_mov_b32 s46, 0x188000\n"
    "s_mov_b32 s47, 0x20000\n"
    "s_and_b32 s53, s53, 0xffff\n"
    "s_mov_b32 s54, 0x1c0000\n"
    "s_mov_b32 s55, 0x20000\n"
    "s_lshl_b32 s61, s68, 2\n"
    "s_add_u32 s61, s61, s67\n"
    "v_lshl_add_u32 v194, v21, 8, s61\n"
    "v_mul_u32_u24 v194, v194, v20\n"
    "v_cvt_f32_u32 v194, v194\n"
    "v_cvt_f32_u32 v192, v20\n"
    "v_mul_f32 v193, 0x3a000000, v194\n"
    "v_mul_f32 v192, 0x3d000000, v192\n"
    "s_lshl_b32 s61, s61, 9\n"
    "buffer_load_dwordx4 v6, s[40:43], s58 offen offset:1024 lds\n"
    "s_add_u32 s61, s61, 135168\n"
    "v_lshl_add_u32 v45, v20, 4, s61\n"
    "v_xor_b32 v198, 512, v45\n"
    "s_waitcnt lgkmcnt(7)\n"
    "v_mfma_f32_32x32x16_bf16 v[112:127], a[224:227], a[0:3], 0\n"
    "ds_read_b64_tr_b16 a[240:241], v18 offset:4096\n"
    "v_cos_f32 v22, v22\n"
    "v_fmamk_f32 v32, v187, 0x42800000, v188\n"
    "v_fmamk_f32 v33, v187, 0x42820000, v188\n"
    "v_mfma_f32_32x32x16_bf16 v[128:143], a[224:227], a[112:115], 0\n"
    "ds_read_b64_tr_b16 a[242:243], v18 offset:4608\n"
    "ds_read_b64_tr_b16 a[244:245], v18 offset:12288\n"
    "v_fmamk_f32 v34, v187, 0x42840000, v188\n"
    "v_fmamk_f32 v35, v187, 0x42860000, v188\n"
    "v_fmamk_f32 v36, v187, 0x42880000, v188\n"
    "v_mfma_f32_32x32x16_bf16 v[144:159], a[228:231], a[0:3], 0\n"
    "ds_read_b64_tr_b16 a[246:247], v18 offset:12800\n"
    "ds_read_b128 a[8:11], v11 offset:0\n"
    "v_fmamk_f32 v37, v187, 0x428a0000, v188\n"
    "buffer_load_dwordx4 v7, s[40:43], s58 offen offset:2048 lds\n"
    "v_mfma_f32_32x32x16_bf16 v[160:175], a[228:231], a[112:115], 0\n"
    "ds_read_b128 v[56:59], v44 offset:2048\n"
    "ds_read_b128 a[12:15], v12 offset:0\n"
    "v_fmamk_f32 v38, v187, 0x428c0000, v188\n"
    "v_fmamk_f32 v39, v187, 0x428e0000, v188\n"
    "s_waitcnt lgkmcnt(7)\n"
    "v_mfma_f32_32x32x16_bf16 v[112:127], a[232:235], a[4:7], v[112:127]\n"
    "ds_read_b64_tr_b16 a[248:249], v18 offset:6144\n"
    "v_cos_f32 v32, v32\n"
    "v_cos_f32 v33, v33\n"
    "v_mfma_f32_32x32x16_bf16 v[128:143], a[232:235], a[116:119], v[128:143]\n"
    "ds_read_b64_tr_b16 a[250:251], v18 offset:6656\n"
    "ds_read_b64_tr_b16 a[252:253], v18 offset:14336\n"
    "v_cos_f32 v34, v34\n"
    "v_cos_f32 v35, v35\n"
    "v_mfma_f32_32x32x16_bf16 v[144:159], a[236:239], a[4:7], v[144:159]\n"
    "ds_read_b64_tr_b16 a[254:255], v18 offset:14848\n"
    "ds_read_b128 v[60:63], v44 offset:3072\n"
    "buffer_load_dwordx4 v8, s[40:43], s58 offen offset:3072 lds\n"
    "v_cos_f32 v36, v36\n"
    "v_mfma_f32_32x32x16_bf16 v[160:175], a[236:239], a[116:119], v[160:175]\n"
    "ds_read_b128 a[120:123], v15 offset:0\n"
    "ds_read_b128 a[124:127], v16 offset:0\n"
    "v_cos_f32 v37, v37\n"
    "v_mfma_f32_32x32x16_bf16 v[96:111], a[0:3], v[48:51], 0\n"
    "v_cos_f32 v38, v38\n"
    "v_cos_f32 v39, v39\n"
    "v_cvt_pk_bf16_f32 v40, v32, v33\n"
    "v_mfma_f32_32x32x16_bf16 v[96:111], a[4:7], v[52:55], v[96:111]\n"
    "v_cvt_pk_bf16_f32 v41, v34, v35\n"
    "v_cvt_pk_bf16_f32 v42, v36, v37\n"
    "v_cvt_pk_bf16_f32 v43, v38, v39\n"
    "s_waitcnt lgkmcnt(8)\n"
    "v_mfma_f32_32x32x16_bf16 v[96:111], a[8:11], v[56:59], v[96:111]\n"
    "s_add_u32 m0, s56, 0xc000\n"
    "s_mov_b32 s57, 0x180\n"
    "s_nop 0\n"
    "buffer_load_dwordx4 v1, s[36:39], s57 offen offset:0 lds\n"
    "ds_write_b128 v239, v[40:43] offset:4096\n"
    "v_add_f32 v22, v22, v22\n"
    "v_sub_f32 v23, 0, v22\n"
    "v_fmac_f32 v24, v23, v32\n"
    "v_fmac_f32 v25, v23, v33\n"
    "v_fmac_f32 v26, v23, v34\n"
    "v_fmac_f32 v27, v23, v35\n"
    "v_fmac_f32 v28, v23, v36\n"
    "v_fmac_f32 v29, v23, v37\n"
    "v_fmac_f32 v30, v23, v38\n"
    "v_fmac_f32 v31, v23, v39\n"
    "v_cvt_pk_bf16_f32 v40, -v24, -v25\n"
    "v_cvt_pk_bf16_f32 v41, -v26, -v27\n"
    "v_cvt_pk_bf16_f32 v42, -v28, -v29\n"
    "v_cvt_pk_bf16_f32 v43, -v30, -v31\n"
    "ds_write_b128 v239, v[40:43] offset:8192\n"
    "s_waitcnt lgkmcnt(4)\n"
    "v_mfma_f32_32x32x16_bf16 v[96:111], a[12:15], v[60:63], v[96:111]\n"
    "v_fmac_f32 v32, v22, v24\n"
    "v_fmac_f32 v33, v22, v25\n"
    "v_fmac_f32 v34, v22, v26\n"
    "v_fmac_f32 v35, v22, v27\n"
    "v_fmac_f32 v36, v22, v28\n"
    "v_fmac_f32 v37, v22, v29\n"
    "v_fmac_f32 v38, v22, v30\n"
    "v_fmac_f32 v39, v22, v31\n"
    "v_cvt_pk_bf16_f32 v40, -v32, -v33\n"
    "v_cvt_pk_bf16_f32 v41, -v34, -v35\n"
    "v_cvt_pk_bf16_f32 v42, -v36, -v37\n"
    "v_cvt_pk_bf16_f32 v43, -v38, -v39\n"
    "ds_write_b128 v239, v[40:43] offset:12288\n"
    "buffer_load_dwordx4 v2, s[36:39], s57 offen offset:1024 lds\n"
    "buffer_load_dwordx4 v3, s[36:39], s57 offen offset:2048 lds\n"
    "s_waitcnt vmcnt(7) lgkmcnt(1)\n"
    "s_barrier\n"
    "ds_read_b128 a[16:19], v9 offset:16384\n"
    "ds_read_b128 v[48:51], v44 offset:4096\n"
    "ds_read_b128 a[20:23], v10 offset:16384\n"
    "ds_read_b128 v[52:55], v44 offset:5120\n"
    "ds_read_b128 a[24:27], v11 offset:16384\n"
    "ds_read_b128 v[56:59], v44 offset:6144\n"
    "ds_read_b128 a[28:31], v12 offset:16384\n"
    "ds_read_b128 v[60:63], v44 offset:7168\n"
    "buffer_load_dwordx4 v4, s[36:39], s57 offen offset:3072 lds\n"
    "s_add_u32 m0, s56, 0x18000\n"
    "s_mov_b32 s58, 0x70000\n"
    "s_nop 0\n"
    "buffer_load_dwordx4 v5, s[40:43], s58 offen offset:0 lds\n"
    "v_fmamk_f32 v223, v192, 0x00000000, v193\n"
    "v_fmamk_f32 v224, v192, 0x3f800000, v193\n"
    "v_fmamk_f32 v225, v192, 0x40000000, v193\n"
    "v_fmamk_f32 v226, v192, 0x40400000, v193\n"
    "v_fmamk_f32 v227, v192, 0x41000000, v193\n"
    "v_fmamk_f32 v228, v192, 0x41100000, v193\n"
    "v_fmamk_f32 v229, v192, 0x41200000, v193\n"
    "v_fmamk_f32 v230, v192, 0x41300000, v193\n"
    "v_fmamk_f32 v231, v192, 0x41800000, v193\n"
    "v_fmamk_f32 v232, v192, 0x41880000, v193\n"
    "s_waitcnt lgkmcnt(6)\n"
    "v_mfma_f32_32x32x16_bf16 v[96:111], a[16:19], v[48:51], v[96:111]\n"
    "ds_read_b128 a[32:35], v9 offset:32768\n"
    "ds_read_b128 v[48:51], v44 offset:8192\n"
    "ds_read_b128 a[128:131], v13 offset:16384\n"
    "buffer_load_dwordx4 v6, s[40:43], s58 offen offset:1024 lds\n"
    "s_waitcnt lgkmcnt(7)\n"
    "v_mfma_f32_32x32x16_bf16 v[96:111], a[20:23], v[52:55], v[96:111]\n"
    "ds_read_b128 a[36:39], v10 offset:32768\n"
    "ds_read_b128 v[52:55], v44 offset:9216\n"
    "ds_read_b128 a[132:135], v14 offset:16384\n"
    "v_fmac_f32 v24, v23, v32\n"
    "s_waitcnt lgkmcnt(8)\n"
    "v_mfma_f32_32x32x16_bf16 v[96:111], a[24:27], v[56:59], v[96:111]\n"
    "ds_read_b128 a[40:43], v11 offset:32768\n"
    "ds_read_b128 v[56:59], v44 offset:10240\n"
    "ds_read_b128 a[136:139], v15 offset:16384\n"
    "v_fmac_f32 v25, v23, v33\n"
    "s_waitcnt lgkmcnt(9)\n"
    "v_mfma_f32_32x32x16_bf16 v[96:111], a[28:31], v[60:63], v[96:111]\n"
    "ds_read_b128 a[44:47], v12 offset:32768\n"
    "ds_read_b128 v[60:63], v44 offset:11264\n"
    "ds_read_b128 a[140:143], v16 offset:16384\n"
    "v_fmac_f32 v26, v23, v34\n"
    "s_waitcnt lgkmcnt(10)\n"
    "v_mfma_f32_32x32x16_bf16 v[96:111], a[32:35], v[48:51], v[96:111]\n"
    "ds_read_b128 a[144:147], v13 offset:32768\n"
    "buffer_load_dwordx4 v7, s[40:43], s58 offen offset:2048 lds\n"
    "v_fmac_f32 v27, v23, v35\n"
    "v_fmac_f32 v28, v23, v36\n"
    "v_fmac_f32 v29, v23, v37\n"
    "v_fmac_f32 v30, v23, v38\n"
    "v_fmac_f32 v31, v23, v39\n"
    "v_cvt_pk_bf16_f32 v40, v24, v25\n"
    "s_waitcnt lgkmcnt(8)\n"
    "v_mfma_f32_32x32x16_bf16 v[96:111], a[36:39], v[52:55], v[96:111]\n"
    "ds_read_b128 a[148:151], v14 offset:32768\n"
    "v_cvt_pk_bf16_f32 v41, v26, v27\n"
    "v_cvt_pk_bf16_f32 v42, v28, v29\n"
    "v_cvt_pk_bf16_f32 v43, v30, v31\n"
    "ds_write_b128 v239, v[40:43] offset:0\n"
    "s_waitcnt lgkmcnt(7)\n"
    "v_mfma_f32_32x32x16_bf16 v[96:111], a[40:43], v[56:59], v[96:111]\n"
    "ds_read_b128 a[152:155], v15 offset:32768\n"
    "v_fmamk_f32 v233, v192, 0x41900000, v193\n"
    "v_fmamk_f32 v234, v192, 0x41980000, v193\n"
    "buffer_load_dwordx4 v8, s[40:43], s58 offen offset:3072 lds\n"
    "v_fmamk_f32 v235, v192, 0x41c00000, v193\n"
    "v_fmamk_f32 v236, v192, 0x41c80000, v193\n"
    "v_fmamk_f32 v237, v192, 0x41d00000, v193\n"
    "s_waitcnt lgkmcnt(5)\n"
    "v_mfma_f32_32x32x16_bf16 v[96:111], a[44:47], v[60:63], v[96:111]\n"
    "ds_read_b128 a[156:159], v16 offset:32768\n"
    "v_fmamk_f32 v238, v192, 0x41d80000, v193\n"
    "v_cos_f32 v207, v223\n"
    "v_cos_f32 v208, v224\n"
    "v_cos_f32 v209, v225\n"
    "v_cos_f32 v210, v226\n"
    "v_cos_f32 v211, v227\n"
    "v_cos_f32 v212, v228\n"
    "v_cos_f32 v213, v229\n"
    "v_cos_f32 v214, v230\n"
    "s_add_u32 m0, s56, 0x0\n"
    "s_mov_b32 s57, 0x200\n"
    "s_nop 0\n"
    "buffer_load_dwordx4 v1, s[36:39], s57 offen offset:0 lds\n"
    "v_cos_f32 v215, v231\n"
    "v_cos_f32 v216, v232\n"
    "v_cos_f32 v217, v233\n"
    "v_cos_f32 v218, v234\n"
    "v_cos_f32 v219, v235\n"
    "v_cos_f32 v220, v236\n"
    "v_cos_f32 v221, v237\n"
    "v_cos_f32 v222, v238\n"
    "v_sin_f32 v223, v223\n"
    "v_sin_f32 v224, v224\n"
    "v_sin_f32 v225, v225\n"
    "v_sin_f32 v226, v226\n"
    "v_sin_f32 v227, v227\n"
    "v_sin_f32 v228, v228\n"
    "buffer_load_dwordx4 v2, s[36:39], s57 offen offset:1024 lds\n"
    "v_sin_f32 v229, v229\n"
    "v_sin_f32 v230, v230\n"
    "v_sin_f32 v231, v231\n"
    "v_sin_f32 v232, v232\n"
    "v_sin_f32 v233, v233\n"
    "v_sin_f32 v234, v234\n"
    "v_sin_f32 v235, v235\n"
    "v_sin_f32 v236, v236\n"
    "v_sin_f32 v237, v237\n"
    "v_sin_f32 v238, v238\n"
    "v_and_b32 v177, 15, v20\n"
    "v_lshlrev_b32 v177, 3, v177\n"
    "v_lshlrev_b32 v178, 2, v21\n"
    "s_lshl_b32 s59, s67, 6\n"
    "v_add_u32 v178, s59, v178\n"
    "v_xor_b32 v178, v178, v177\n"
    "buffer_load_dwordx4 v3, s[36:39], s57 offen offset:2048 lds\n"
    "v_lshl_add_u32 v178, v20, 8, v178\n"
    "v_add_u32 v201, 49152, v178\n"
    "v_lshlrev_b32 v178, 10, v21\n"
    "v_lshl_add_u32 v178, v20, 5, v178\n"
    "s_lshl_b32 s59, s67, 1\n"
    "s_add_u32 s60, s59, 57344\n"
    "v_add_u32 v202, s60, v178\n"
    "s_lshl_b32 s59, s67, 3\n"
    "v_lshrrev_b32 v177, 4, v0\n"
    "v_add_u32 v177, s59, v177\n"
    "v_and_b32 v178, 14, v0\n"
    "v_lshlrev_b32 v178, 4, v178\n"
    "v_and_b32 v179, 1, v0\n"
    "v_lshl_add_u32 v178, v179, 3, v178\n"
    "v_and_b32 v179, 15, v177\n"
    "v_lshlrev_b32 v179, 3, v179\n"
    "v_xor_b32 v178, v178, v179\n"
    "v_lshl_add_u32 v178, v177, 8, v178\n"
    "v_add_u32 v203, 49152, v178\n"
    "v_bfe_u32 v179, v0, 3, 1\n"
    "v_lshlrev_b32 v179, 4, v179\n"
    "v_lshl_add_u32 v179, v177, 5, v179\n"
    "v_add_u32 v204, 57344, v179\n"
    "v_and_b32 v178, 15, v0\n"
    "v_lshlrev_b32 v178, 4, v178\n"
    "v_mul_u32_u24 v177, 66816, v177\n"
    "v_add_u32 v178, v177, v178\n"
    "s_lshl_b32 s59, s68, 8\n"
    "s_add_u32 s59, s59, 1048576\n"
    "s_lshl_b32 s60, s69, 12\n"
    "s_add_u32 s59, s59, s60\n"
    "v_add_u32 v205, s59, v178\n"
    "v_add_u32 v206, 57344, v205\n"
    "s_mov_b32 s74, 267264\n"
    "v_lshl_add_u32 v200, s69, 6, v0\n"
    "v_cmp_eq_u32 vcc, 0, v200\n"
    "v_mov_b32 v92, 0x43e0\n"
    "v_mov_b32 v93, 0\n"
    "v_cndmask_b32 v92, 0, v92, vcc\n"
    "v_mov_b32 v94, 0\n"
    "v_mov_b32 v95, 0\n"
    "s_waitcnt vmcnt(7) lgkmcnt(0)\n"
    "s_barrier\n"
    "buffer_load_dwordx4 v4, s[36:39], s57 offen offset:3072 lds\n"
    "v_mfma_f32_32x32x16_bf16 v[112:127], a[240:243], a[8:11], v[112:127]\n"
    "ds_read_b64_tr_b16 a[224:225], v18 offset:16384\n"
    "ds_read_b64_tr_b16 a[226:227], v18 offset:16896\n"
    "v_fmac_f32 v32, v22, v24\n"
    "v_fmac_f32 v33, v22, v25\n"
    "v_fmac_f32 v34, v22, v26\n"
    "v_fmac_f32 v35, v22, v27\n"
    "v_mfma_f32_32x32x16_bf16 v[128:143], a[240:243], a[120:123], v[128:143]\n"
    "ds_read_b64_tr_b16 a[228:229], v18 offset:24576\n"
    "ds_read_b64_tr_b16 a[230:231], v18 offset:25088\n"
    "ds_read_b128 a[48:51], v9 offset:49152\n"
    "s_add_u32 m0, s56, 0x4000\n"
    "s_mov_b32 s57, 0x280\n"
    "s_nop 0\n"
    "buffer_load_dwordx4 v1, s[36:39], s57 offen offset:0 lds\n"
    "v_mfma_f32_32x32x16_bf16 v[144:159], a[244:247], a[8:11], v[144:159]\n"
    "ds_read_b128 v[48:51], v44 offset:12288\n"
    "ds_read_b128 a[52:55], v10 offset:49152\n"
    "v_fmac_f32 v36, v22, v28\n"
    "v_fmac_f32 v37, v22, v29\n"
    "v_fmac_f32 v38, v22, v30\n"
    "v_mfma_f32_32x32x16_bf16 v[160:175], a[244:247], a[120:123], v[160:175]\n"
    "ds_read_b128 v[52:55], v44 offset:13312\n"
    "ds_read_b128 a[56:59], v11 offset:49152\n"
    "ds_read_b128 v[56:59], v44 offset:14336\n"
    "v_fmac_f32 v39, v22, v31\n"
    "v_mfma_f32_32x32x16_bf16 v[112:127], a[248:251], a[12:15], v[112:127]\n"
    "ds_read_b64_tr_b16 a[232:233], v18 offset:18432\n"
    "ds_read_b64_tr_b16 a[234:235], v18 offset:18944\n"
    "v_cvt_pk_bf16_f32 v40, v32, v33\n"
    "buffer_load_dwordx4 v2, s[36:39], s57 offen offset:1024 lds\n"
    "v_cvt_pk_bf16_f32 v41, v34, v35\n"
    "v_mfma_f32_32x32x16_bf16 v[128:143], a[248:251], a[124:127], v[128:143]\n"
    "ds_read_b64_tr_b16 a[236:237], v18 offset:26624\n"
    "ds_read_b64_tr_b16 a[238:239], v18 offset:27136\n"
    "ds_read_b128 a[60:63], v12 offset:49152\n"
    "v_cvt_pk_bf16_f32 v42, v36, v37\n"
    "v_cvt_pk_bf16_f32 v43, v38, v39\n"
    "v_mfma_f32_32x32x16_bf16 v[144:159], a[252:255], a[12:15], v[144:159]\n"
    "s_waitcnt lgkmcnt(13)\n"
    "ds_read_b128 v[60:63], v44 offset:15360\n"
    "ds_read_b128 a[160:163], v13 offset:49152\n"
    "s_waitcnt lgkmcnt(13)\n"
    "ds_write_b128 v239, v[40:43] offset:4096\n"
    "v_mfma_f32_32x32x16_bf16 v[160:175], a[252:255], a[124:127], v[160:175]\n"
    "ds_read_b128 a[164:167], v14 offset:49152\n"
    "s_waitcnt lgkmcnt(13)\n"
    "ds_read_b128 a[168:171], v15 offset:49152\n"
    "ds_read_b128 a[172:175], v16 offset:49152\n"
    "buffer_load_dwordx4 v3, s[36:39], s57 offen offset:2048 lds\n"
    "v_mfma_f32_32x32x16_bf16 v[112:127], a[224:227], a[16:19], v[112:127]\n"
    "s_waitcnt lgkmcnt(13)\n"
    "ds_read_b64_tr_b16 a[240:241], v18 offset:20480\n"
    "v_fmac_f32 v24, v23, v32\n"
    "v_fmac_f32 v25, v23, v33\n"
    "v_fmac_f32 v26, v23, v34\n"
    "v_fmac_f32 v27, v23, v35\n"
    "v_fmac_f32 v28, v23, v36\n"
    "v_mfma_f32_32x32x16_bf16 v[128:143], a[224:227], a[128:131], v[128:143]\n"
    "ds_read_b64_tr_b16 a[242:243], v18 offset:20992\n"
    "v_fmac_f32 v29, v23, v37\n"
    "v_fmac_f32 v30, v23, v38\n"
    "v_fmac_f32 v31, v23, v39\n"
    "v_cvt_pk_bf16_f32 v40, -v24, -v25\n"
    "v_mfma_f32_32x32x16_bf16 v[144:159], a[228:231], a[16:19], v[144:159]\n"
    "s_waitcnt lgkmcnt(13)\n"
    "ds_read_b64_tr_b16 a[244:245], v18 offset:28672\n"
    "v_cvt_pk_bf16_f32 v41, -v26, -v27\n"
    "v_cvt_pk_bf16_f32 v42, -v28, -v29\n"
    "v_cvt_pk_bf16_f32 v43, -v30, -v31\n"
    "v_mfma_f32_32x32x16_bf16 v[160:175], a[228:231], a[128:131], v[160:175]\n"
    "ds_read_b64_tr_b16 a[246:247], v18 offset:29184\n"
    "buffer_load_dwordx4 v4, s[36:39], s57 offen offset:3072 lds\n"
    "s_waitcnt lgkmcnt(13)\n"
    "ds_write_b128 v239, v[40:43] offset:8192\n"
    "v_mfma_f32_32x32x16_bf16 v[96:111], a[48:51], v[48:51], v[96:111]\n"
    "v_mfma_f32_32x32x16_bf16 v[96:111], a[52:55], v[52:55], v[96:111]\n"
    "s_waitcnt lgkmcnt(12)\n"
    "v_mfma_f32_32x32x16_bf16 v[112:127], a[232:235], a[20:23], v[112:127]\n"
    "ds_read_b64_tr_b16 a[248:249], v18 offset:22528\n"
    "buffer_load_dwordx4 v[64:67], v45, s[48:51], 0 offen offset:0 sc1\n"
    "v_mfma_f32_32x32x16_bf16 v[128:143], a[232:235], a[132:135], v[128:143]\n"
    "ds_read_b64_tr_b16 a[250:251], v18 offset:23040\n"
    "buffer_load_dwordx4 v[68:71], v198, s[48:51], 0 offen offset:0 sc1\n"
    "v_mfma_f32_32x32x16_bf16 v[144:159], a[236:239], a[20:23], v[144:159]\n"
    "ds_read_b64_tr_b16 a[252:253], v18 offset:30720\n"
    "v_mfma_f32_32x32x16_bf16 v[160:175], a[236:239], a[132:135], v[160:175]\n"
    "s_waitcnt lgkmcnt(13)\n"
    "ds_read_b64_tr_b16 a[254:255], v18 offset:31232\n"
    "v_mfma_f32_32x32x16_bf16 v[96:111], a[56:59], v[56:59], v[96:111]\n"
    "s_add_u32 m0, s56, 0x8000\n"
    "s_mov_b32 s57, 0x300\n"
    "s_nop 0\n"
    "buffer_load_dwordx4 v1, s[36:39], s57 offen offset:0 lds\n"
    "v_mfma_f32_32x32x16_bf16 v[96:111], a[60:63], v[60:63], v[96:111]\n"
    "buffer_load_dwordx4 v2, s[36:39], s57 offen offset:1024 lds\n"
    "s_waitcnt vmcnt(8) lgkmcnt(9)\n"
    "s_barrier\n"
    "buffer_load_dwordx4 v3, s[36:39], s57 offen offset:2048 lds\n"
    "s_waitcnt lgkmcnt(5)\n"
    "v_mfma_f32_32x32x16_bf16 v[112:127], a[240:243], a[24:27], v[112:127]\n"
    "ds_read_b64_tr_b16 a[224:225], v18 offset:32768\n"
    "ds_read_b64_tr_b16 a[226:227], v18 offset:33280\n"
    "v_mfma_f32_32x32x16_bf16 v[128:143], a[240:243], a[136:139], v[128:143]\n"
    "ds_read_b64_tr_b16 a[228:229], v18 offset:40960\n"
    "ds_read_b64_tr_b16 a[230:231], v18 offset:41472\n"
    "ds_read_b128 a[64:67], v9 offset:0\n"
    "buffer_load_dwordx4 v4, s[36:39], s57 offen offset:3072 lds\n"
    "v_mfma_f32_32x32x16_bf16 v[144:159], a[244:247], a[24:27], v[144:159]\n"
    "ds_read_b128 v[48:51], v44 offset:0\n"
    "ds_read_b128 a[68:71], v10 offset:0\n"
    "v_mfma_f32_32x32x16_bf16 v[160:175], a[244:247], a[136:139], v[160:175]\n"
    "ds_read_b128 v[52:55], v44 offset:1024\n"
    "ds_read_b128 a[72:75], v11 offset:0\n"
    "ds_read_b128 v[56:59], v44 offset:2048\n"
    "s_waitcnt lgkmcnt(10)\n"
    "v_mfma_f32_32x32x16_bf16 v[112:127], a[248:251], a[28:31], v[112:127]\n"
    "ds_read_b64_tr_b16 a[232:233], v18 offset:34816\n"
    "ds_read_b64_tr_b16 a[234:235], v18 offset:35328\n"
    "s_add_u32 m0, s56, 0x1c000\n"
    "s_mov_b32 s58, 0xa8000\n"
    "s_nop 0\n"
    "buffer_load_dwordx4 v5, s[40:43], s58 offen offset:0 lds\n"
    "v_mfma_f32_32x32x16_bf16 v[128:143], a[248:251], a[140:143], v[128:143]\n"
    "ds_read_b64_tr_b16 a[236:237], v18 offset:43008\n"
    "ds_read_b64_tr_b16 a[238:239], v18 offset:43520\n"
    "ds_read_b128 a[76:79], v12 offset:0\n"
    "v_mfma_f32_32x32x16_bf16 v[144:159], a[252:255], a[28:31], v[144:159]\n"
    "s_waitcnt lgkmcnt(13)\n"
    "ds_read_b128 v[60:63], v44 offset:3072\n"
    "ds_read_b128 a[176:179], v13 offset:0\n"
    "v_mfma_f32_32x32x16_bf16 v[160:175], a[252:255], a[140:143], v[160:175]\n"
    "s_waitcnt lgkmcnt(13)\n"
    "ds_read_b128 a[180:183], v14 offset:0\n"
    "ds_read_b128 a[184:187], v15 offset:0\n"
    "s_waitcnt lgkmcnt(13)\n"
    "ds_read_b128 a[188:191], v16 offset:0\n"
    "v_mfma_f32_32x32x16_bf16 v[112:127], a[224:227], a[32:35], v[112:127]\n"
    "ds_read_b64_tr_b16 a[240:241], v18 offset:36864\n"
    "buffer_load_dwordx4 v6, s[40:43], s58 offen offset:1024 lds\n"
    "v_mfma_f32_32x32x16_bf16 v[128:143], a[224:227], a[144:147], v[128:143]\n"
    "s_waitcnt lgkmcnt(13)\n"
    "ds_read_b64_tr_b16 a[242:243], v18 offset:37376\n"
    "v_mfma_f32_32x32x16_bf16 v[144:159], a[228:231], a[32:35], v[144:159]\n"
    "ds_read_b64_tr_b16 a[244:245], v18 offset:45056\n"
    "v_mfma_f32_32x32x16_bf16 v[160:175], a[228:231], a[144:147], v[160:175]\n"
    "s_waitcnt lgkmcnt(13)\n"
    "ds_read_b64_tr_b16 a[246:247], v18 offset:45568\n"
    "buffer_load_dwordx4 v7, s[40:43], s58 offen offset:2048 lds\n"
    "v_mfma_f32_32x32x16_bf16 v[96:111], a[64:67], v[48:51], v[96:111]\n"
    "v_mfma_f32_32x32x16_bf16 v[96:111], a[68:71], v[52:55], v[96:111]\n"
    "s_waitcnt lgkmcnt(10)\n"
    "v_mfma_f32_32x32x16_bf16 v[112:127], a[232:235], a[36:39], v[112:127]\n"
    "ds_read_b64_tr_b16 a[248:249], v18 offset:38912\n"
    "v_mfma_f32_32x32x16_bf16 v[128:143], a[232:235], a[148:151], v[128:143]\n"
    "ds_read_b64_tr_b16 a[250:251], v18 offset:39424\n"
    "buffer_load_dwordx4 v8, s[40:43], s58 offen offset:3072 lds\n"
    "s_waitcnt vmcnt(8)\n"
    "v_cmp_ne_u32 vcc, s62, v66\n"
    "v_cmp_ne_u32 s[64:65], s63, v67\n"
    "s_nop 3\n"
    "s_or_b64 vcc, vcc, s[64:65]\n"
    "v_cmp_ne_u32 s[64:65], s62, v70\n"
    "v_mfma_f32_32x32x16_bf16 v[144:159], a[236:239], a[36:39], v[144:159]\n"
    "ds_read_b64_tr_b16 a[252:253], v18 offset:47104\n"
    "s_nop 2\n"
    "s_or_b64 vcc, vcc, s[64:65]\n"
    "v_cmp_ne_u32 s[64:65], s63, v71\n"
    "s_nop 3\n"
    "s_or_b64 vcc, vcc, s[64:65]\n"
    "s_cbranch_vccnz .Lm_st_slow\n"
    ".Lm_st_ok:\n"
    "s_lshl_b32 s59, s67, 7\n"
    "s_add_u32 s59, s59, 59392\n"
    "v_lshl_add_u32 v176, v20, 2, s59\n"
    "v_mfma_f32_32x32x16_bf16 v[160:175], a[236:239], a[148:151], v[160:175]\n"
    "ds_read_b64_tr_b16 a[254:255], v18 offset:47616\n"
    "ds_write_b32 v176, v65 offset:0\n"
    "v_lshl_add_u32 v176, v21, 4, s59\n"
    "v_cmp_eq_u32 vcc, 0, v21\n"
    "s_mov_b32 s60, 0x01000100\n"
    "v_mfma_f32_32x32x16_bf16 v[96:111], a[72:75], v[56:59], v[96:111]\n"
    "s_waitcnt lgkmcnt(13)\n"
    "ds_read_b128 v[76:79], v176 offset:0\n"
    "ds_read_b128 v[80:83], v176 offset:32\n"
    "s_waitcnt lgkmcnt(13)\n"
    "ds_read_b128 v[84:87], v176 offset:64\n"
    "v_mfma_f32_32x32x16_bf16 v[96:111], a[76:79], v[60:63], v[96:111]\n"
    "s_add_u32 m0, s56, 0x20000\n"
    "s_mov_b32 s58, 0xe0000\n"
    "s_nop 0\n"
    "buffer_load_dwordx4 v5, s[40:43], s58 offen offset:0 lds\n"
    "ds_read_b128 v[88:91], v176 offset:96\n"
    "v_mul_f32 v72, 0xbfb8aa3b, v65\n"
    "v_and_b32 v74, 0xffff0000, v64\n"
    "v_perm_b32 v242, v64, v64, s60\n"
    "v_mul_f32 v74, v74, v74\n"
    "v_xor_b32 v242, 0x80008000, v242\n"
    "v_cndmask_b32 v242, 0, v242, vcc\n"
    "v_mov_b32 v240, 0\n"
    "v_mov_b32 v241, 0\n"
    "v_mov_b32 v243, 0\n"
    "v_mov_b32 v244, 0\n"
    "v_mov_b32 v245, 0\n"
    "v_mul_f32 v73, 0xbfb8aa3b, v69\n"
    "v_and_b32 v75, 0xffff0000, v68\n"
    "v_perm_b32 v248, v68, v68, s60\n"
    "v_mul_f32 v75, v75, v75\n"
    "v_xor_b32 v248, 0x80008000, v248\n"
    "v_cndmask_b32 v248, 0, v248, vcc\n"
    "v_mov_b32 v246, 0\n"
    "v_mov_b32 v247, 0\n"
    "buffer_load_dwordx4 v6, s[40:43], s58 offen offset:1024 lds\n"
    "v_mov_b32 v249, 0\n"
    "v_mov_b32 v250, 0\n"
    "v_mov_b32 v251, 0\n"
    "s_lshl_b32 s59, s69, 7\n"
    "s_and_b32 s60, s67, 1\n"
    "s_lshl_b32 s60, s60, 5\n"
    "s_add_u32 s59, s59, s60\n"
    "s_lshl_b32 s59, s59, 4\n"
    "s_add_u32 s59, s59, 106496\n"
    "v_lshl_add_u32 v65, v20, 4, s59\n"
    "s_waitcnt lgkmcnt(0)\n"
    "v_mul_f32 v207, v207, v76\n"
    "v_mul_f32 v208, v208, v77\n"
    "v_mul_f32 v209, v209, v78\n"
    "v_mul_f32 v210, v210, v79\n"
    "v_mul_f32 v211, v211, v80\n"
    "v_mul_f32 v212, v212, v81\n"
    "v_mul_f32 v213, v213, v82\n"
    "v_mul_f32 v214, v214, v83\n"
    "v_mul_f32 v215, v215, v84\n"
    "v_mul_f32 v216, v216, v85\n"
    "v_mul_f32 v217, v217, v86\n"
    "v_mul_f32 v218, v218, v87\n"
    "v_mul_f32 v219, v219, v88\n"
    "v_mul_f32 v220, v220, v89\n"
    "v_mul_f32 v221, v221, v90\n"
    "v_mul_f32 v222, v222, v91\n"
    "v_mul_f32 v223, v223, v76\n"
    "v_mul_f32 v224, v224, v77\n"
    "v_mul_f32 v225, v225, v78\n"
    "v_mul_f32 v226, v226, v79\n"
    "v_mul_f32 v227, v227, v80\n"
    "v_mul_f32 v228, v228, v81\n"
    "v_mul_f32 v229, v229, v82\n"
    "v_mul_f32 v230, v230, v83\n"
    "buffer_load_dwordx4 v7, s[40:43], s58 offen offset:2048 lds\n"
    "v_mul_f32 v231, v231, v84\n"
    "v_mul_f32 v232, v232, v85\n"
    "v_mul_f32 v233, v233, v86\n"
    "v_mul_f32 v234, v234, v87\n"
    "v_mul_f32 v235, v235, v88\n"
    "v_mul_f32 v236, v236, v89\n"
    "v_mul_f32 v237, v237, v90\n"
    "v_mul_f32 v238, v238, v91\n"
    "v_cvt_pk_bf16_f32 v76, v207, v208\n"
    "v_cvt_pk_bf16_f32 v77, v209, v210\n"
    "v_cvt_pk_bf16_f32 v78, v211, v212\n"
    "v_cvt_pk_bf16_f32 v79, v213, v214\n"
    "v_cvt_pk_bf16_f32 v80, v215, v216\n"
    "v_cvt_pk_bf16_f32 v81, v217, v218\n"
    "v_cvt_pk_bf16_f32 v82, v219, v220\n"
    "v_cvt_pk_bf16_f32 v83, v221, v222\n"
    "v_cvt_pk_bf16_f32 v84, v223, v224\n"
    "v_cvt_pk_bf16_f32 v85, v225, v226\n"
    "v_cvt_pk_bf16_f32 v86, v227, v228\n"
    "v_cvt_pk_bf16_f32 v87, v229, v230\n"
    "v_cvt_pk_bf16_f32 v88, v231, v232\n"
    "v_cvt_pk_bf16_f32 v89, v233, v234\n"
    "v_cvt_pk_bf16_f32 v90, v235, v236\n"
    "s_waitcnt vmcnt(7)\n"
    "s_barrier\n"
    "ds_read_b128 a[80:83], v9 offset:16384\n"
    "ds_read_b128 v[48:51], v44 offset:4096\n"
    "ds_read_b128 a[84:87], v10 offset:16384\n"
    "ds_read_b128 v[52:55], v44 offset:5120\n"
    "ds_read_b128 a[88:91], v11 offset:16384\n"
    "ds_read_b128 v[56:59], v44 offset:6144\n"
    "ds_read_b128 a[92:95], v12 offset:16384\n"
    "ds_read_b128 v[60:63], v44 offset:7168\n"
    "buffer_load_dwordx4 v8, s[40:43], s58 offen offset:3072 lds\n"
    "s_add_u32 m0, s56, 0x10000\n"
    "s_mov_b32 s58, 0x118000\n"
    "s_nop 0\n"
    "buffer_load_dwordx4 v5, s[40:43], s58 offen offset:0 lds\n"
    "v_cvt_pk_bf16_f32 v91, v237, v238\n"
    "s_waitcnt lgkmcnt(6)\n"
    "v_mfma_f32_32x32x16_bf16 v[96:111], a[80:83], v[48:51], v[96:111]\n"
    "ds_read_b128 a[96:99], v9 offset:32768\n"
    "ds_read_b128 v[48:51], v44 offset:8192\n"
    "ds_read_b128 a[192:195], v13 offset:16384\n"
    "buffer_load_dwordx4 v6, s[40:43], s58 offen offset:1024 lds\n"
    "s_waitcnt lgkmcnt(7)\n"
    "v_mfma_f32_32x32x16_bf16 v[96:111], a[84:87], v[52:55], v[96:111]\n"
    "ds_read_b128 a[100:103], v10 offset:32768\n"
    "ds_read_b128 v[52:55], v44 offset:9216\n"
    "ds_read_b128 a[196:199], v14 offset:16384\n"
    "s_waitcnt lgkmcnt(8)\n"
    "v_mfma_f32_32x32x16_bf16 v[96:111], a[88:91], v[56:59], v[96:111]\n"
    "ds_read_b128 a[104:107], v11 offset:32768\n"
    "ds_read_b128 v[56:59], v44 offset:10240\n"
    "ds_read_b128 a[200:203], v15 offset:16384\n"
    "s_waitcnt lgkmcnt(9)\n"
    "v_mfma_f32_32x32x16_bf16 v[96:111], a[92:95], v[60:63], v[96:111]\n"
    "ds_read_b128 a[108:111], v12 offset:32768\n"
    "ds_read_b128 v[60:63], v44 offset:11264\n"
    "ds_read_b128 a[204:207], v16 offset:16384\n"
    "s_waitcnt lgkmcnt(10)\n"
    "v_mfma_f32_32x32x16_bf16 v[96:111], a[96:99], v[48:51], v[96:111]\n"
    "ds_read_b128 a[208:211], v13 offset:32768\n"
    "buffer_load_dwordx4 v7, s[40:43], s58 offen offset:2048 lds\n"
    "s_waitcnt lgkmcnt(8)\n"
    "v_mfma_f32_32x32x16_bf16 v[96:111], a[100:103], v[52:55], v[96:111]\n"
    "ds_read_b128 a[212:215], v14 offset:32768\n"
    "s_waitcnt lgkmcnt(6)\n"
    "v_mfma_f32_32x32x16_bf16 v[96:111], a[104:107], v[56:59], v[96:111]\n"
    "ds_read_b128 a[216:219], v15 offset:32768\n"
    "ds_read_b128 a[220:223], v16 offset:32768\n"
    "buffer_load_dwordx4 v8, s[40:43], s58 offen offset:3072 lds\n"
    "v_mfma_f32_32x32x16_bf16 v[96:111], v[242:245], v[92:95], v[96:111]\n"
    "s_waitcnt lgkmcnt(5)\n"
    "v_mfma_f32_32x32x16_bf16 v[96:111], a[108:111], v[60:63], v[96:111]\n"
    "s_nop 13\n"
    "v_cvt_pk_bf16_f32 v48, v96, v97\n"
    "v_cvt_pk_bf16_f32 v49, v98, v99\n"
    "s_add_u32 m0, s56, 0x0\n"
    "s_mov_b32 s58, 0x150000\n"
    "s_nop 0\n"
    "buffer_load_dwordx4 v5, s[40:43], s58 offen offset:0 lds\n"
    "v_cvt_pk_bf16_f32 v50, v100, v101\n"
    "v_cvt_pk_bf16_f32 v51, v102, v103\n"
    "v_cvt_pk_bf16_f32 v52, v104, v105\n"
    "v_cvt_pk_bf16_f32 v53, v106, v107\n"
    "v_cvt_pk_bf16_f32 v54, v108, v109\n"
    "v_cvt_pk_bf16_f32 v55, v110, v111\n"
    "v_mfma_f32_32x32x16_bf16 v[96:111], v[48:51], v[76:79], 0\n"
    "v_mfma_f32_32x32x16_bf16 v[24:39], v[48:51], v[84:87], 0\n"
    "v_mfma_f32_32x32x16_bf16 v[96:111], v[52:55], v[80:83], v[96:111]\n"
    "buffer_load_dwordx4 v6, s[40:43], s58 offen offset:1024 lds\n"
    "v_mfma_f32_32x32x16_bf16 v[24:39], v[52:55], v[88:91], v[24:39]\n"
    "s_nop 13\n"
    "v_cvt_pk_bf16_f32 v48, v96, -v24\n"
    "v_cvt_pk_bf16_f32 v49, v104, -v32\n"
    "ds_write_b16 v202, v48 offset:0\n"
    "ds_write_b16_d16_hi v202, v48 offset:16\n"
    "ds_write_b16 v202, v49 offset:8\n"
    "ds_write_b16_d16_hi v202, v49 offset:24\n"
    "v_add_f32 v96, v96, v32\n"
    "buffer_load_dwordx4 v7, s[40:43], s58 offen offset:2048 lds\n"
    "v_sub_f32 v104, v104, v24\n"
    "v_add_f32 v97, v97, v33\n"
    "v_sub_f32 v105, v105, v25\n"
    "v_add_f32 v98, v98, v34\n"
    "v_sub_f32 v106, v106, v26\n"
    "v_add_f32 v99, v99, v35\n"
    "v_sub_f32 v107, v107, v27\n"
    "v_add_f32 v100, v100, v36\n"
    "v_sub_f32 v108, v108, v28\n"
    "v_add_f32 v101, v101, v37\n"
    "v_sub_f32 v109, v109, v29\n"
    "v_add_f32 v102, v102, v38\n"
    "v_sub_f32 v110, v110, v30\n"
    "v_add_f32 v103, v103, v39\n"
    "v_sub_f32 v111, v111, v31\n"
    "v_cvt_pk_bf16_f32 v56, v96, v98\n"
    "ds_write_b32 v201, v56 offset:0\n"
    "v_cvt_pk_bf16_f32 v57, v97, v99\n"
    "v_xor_b32 v201, 16, v201\n"
    "ds_write_b32 v201, v57 offset:0\n"
    "v_cvt_pk_bf16_f32 v58, v100, v102\n"
    "v_xor_b32 v201, 24, v201\n"
    "ds_write_b32 v201, v58 offset:0\n"
    "v_cvt_pk_bf16_f32 v59, v101, v103\n"
    "v_xor_b32 v201, 16, v201\n"
    "ds_write_b32 v201, v59 offset:0\n"
    "v_cvt_pk_bf16_f32 v60, v104, v106\n"
    "v_xor_b32 v201, 56, v201\n"
    "ds_write_b32 v201, v60 offset:0\n"
    "v_cvt_pk_bf16_f32 v61, v105, v107\n"
    "v_xor_b32 v201, 16, v201\n"
    "ds_write_b32 v201, v61 offset:0\n"
    "v_cvt_pk_bf16_f32 v62, v108, v110\n"
    "v_xor_b32 v201, 24, v201\n"
    "s_waitcnt lgkmcnt(13)\n"
    "ds_write_b32 v201, v62 offset:0\n"
    "v_cvt_pk_bf16_f32 v63, v109, v111\n"
    "v_xor_b32 v201, 16, v201\n"
    "ds_write_b32 v201, v63 offset:0\n"
    "buffer_load_dwordx4 v8, s[40:43], s58 offen offset:3072 lds\n"
    "s_waitcnt vmcnt(8) lgkmcnt(0)\n"
    "s_barrier\n"
    "ds_read_b64_tr_b16 v[24:25], v203 offset:0\n"
    "v_xor_b32 v203, 16, v203\n"
    "ds_read_b64_tr_b16 v[26:27], v203 offset:0\n"
    "v_xor_b32 v203, 32, v203\n"
    "ds_read_b64_tr_b16 v[30:31], v203 offset:1024\n"
    "v_xor_b32 v203, 16, v203\n"
    "ds_read_b64_tr_b16 v[28:29], v203 offset:1024\n"
    "ds_read_b128 v[32:35], v204 offset:0\n"
    "ds_read_b128 v[36:39], v204 offset:128\n"
    "s_add_u32 m0, s56, 0x14000\n"
    "s_mov_b32 s58, 0x80\n"
    "s_nop 0\n"
    "buffer_load_dwordx4 v5, s[40:43], s58 offen offset:0 lds\n"
    "v_mfma_f32_32x32x16_bf16 v[112:127], a[240:243], a[40:43], v[112:127]\n"
    "ds_read_b64_tr_b16 a[224:225], v18 offset:49152\n"
    "buffer_load_dwordx4 v6, s[40:43], s58 offen offset:1024 lds\n"
    "v_mfma_f32_32x32x16_bf16 v[128:143], a[240:243], a[152:155], v[128:143]\n"
    "ds_read_b64_tr_b16 a[226:227], v18 offset:49664\n"
    "v_mfma_f32_32x32x16_bf16 v[144:159], a[244:247], a[40:43], v[144:159]\n"
    "ds_read_b64_tr_b16 a[228:229], v18 offset:57344\n"
    "v_mfma_f32_32x32x16_bf16 v[160:175], a[244:247], a[152:155], v[160:175]\n"
    "ds_read_b64_tr_b16 a[230:231], v18 offset:57856\n"
    "s_waitcnt lgkmcnt(4)\n"
    "buffer_store_dwordx4 v[24:27], v205, s[48:51], 0 offen offset:0 sc1\n"
    "v_mfma_f32_32x32x16_bf16 v[112:127], a[248:251], a[44:47], v[112:127]\n"
    "ds_read_b64_tr_b16 a[232:233], v18 offset:51200\n"
    "buffer_load_dwordx4 v7, s[40:43], s58 offen offset:2048 lds\n"
    "buffer_store_dwordx4 v[28:31], v205, s[48:51], s74 offen offset:0 sc1\n"
    "s_cmp_lg_u32 s69, 0\ns_cbranch_scc1 .Lm_nosp\n"
    "buffer_store_dwordx4 v[32:35], v206, s[48:51], 0 offen offset:0 sc1\n"
    "buffer_store_dwordx4 v[36:39], v206, s[48:51], s74 offen offset:0 sc1\n"
    ".Lm_nosp:\n"
    "v_mfma_f32_32x32x16_bf16 v[128:143], a[248:251], a[156:159], v[128:143]\n"
    "ds_read_b64_tr_b16 a[234:235], v18 offset:51712\n"
    "s_lshl_b32 s60, s68, 2\n"
    "s_add_u32 s59, s67, s60\n"
    "s_mul_i32 s59, s59, 112\n"
    "buffer_load_dwordx4 v8, s[40:43], s58 offen offset:3072 lds\n"
    "s_lshl_b32 s60, s69, 3\n"
    "s_add_u32 s59, s59, s60\n"
    "s_and_b32 s60, s67, 1\n"
    "s_lshl_b32 s61, s60, 1\n"
    "s_add_u32 s59, s59, s61\n"
    "s_lshl_b32 s59, s59, 10\n"
    "v_mfma_f32_32x32x16_bf16 v[144:159], a[252:255], a[44:47], v[144:159]\n"
    "ds_read_b64_tr_b16 a[236:237], v18 offset:59392\n"
    "s_add_u32 s59, s59, 6291456\n"
    "v_lshl_add_u32 v9, v0, 4, s59\n"
    "s_mov_b32 s61, 114688\n"
    "s_cmp_eq_u32 s60, 0\ns_cselect_b32 s60, s61, -114688\n"
    "v_add_u32 v64, s60, v9\n"
    "s_lshl_b32 s59, s72, 2\n"
    "s_add_u32 s59, s59, s67\n"
    "s_lshl_b32 s59, s59, 7\n"
    "v_mfma_f32_32x32x16_bf16 v[160:175], a[252:255], a[156:159], v[160:175]\n"
    "ds_read_b64_tr_b16 a[238:239], v18 offset:59904\n"
    "v_cmp_eq_u32 vcc, 0, v0\n"
    "v_mov_b32 v69, 0x7ffffff0\n"
    "v_mov_b32 v70, s59\n"
    "v_cndmask_b32 v16, v69, v70, vcc\n"
    "s_mov_b32 s73, 4096\n"
    "s_waitcnt lgkmcnt(4)\n"
    "v_mfma_f32_32x32x16_bf16 v[112:127], a[224:227], a[48:51], v[112:127]\n"
    "ds_read_b64_tr_b16 a[240:241], v18 offset:53248\n"
    "s_lshl_b32 s59, s67, 12\n"
    "s_and_b32 s60, s67, 1\n"
    "s_lshl_b32 s60, s60, 11\n"
    "s_add_u32 s59, s59, s60\n"
    "s_add_u32 s59, s59, 147456\n"
    "v_lshl_add_u32 v13, v0, 4, s59\n"
    "s_mul_i32 s59, s70, 896\n"
    "s_lshl_b32 s60, s67, 5\n"
    "s_add_u32 s59, s59, s60\n"
    "s_mul_i32 s59, s59, 896\n"
    "s_lshl_b32 s60, s71, 7\n"
    "s_add_u32 s59, s59, s60\n"
    "v_mfma_f32_32x32x16_bf16 v[128:143], a[224:227], a[160:163], v[128:143]\n"
    "ds_read_b64_tr_b16 a[242:243], v18 offset:53760\n"
    "s_add_u32 m0, s56, 0x4000\n"
    "s_mov_b32 s58, 0x38080\n"
    "s_nop 0\n"
    "buffer_load_dwordx4 v5, s[40:43], s58 offen offset:0 lds\n"
    "v_lshrrev_b32 v200, 3, v0\n"
    "v_mul_u32_u24 v200, 896, v200\n"
    "v_mfma_f32_32x32x16_bf16 v[144:159], a[228:231], a[48:51], v[144:159]\n"
    "ds_read_b64_tr_b16 a[244:245], v18 offset:61440\n"
    "v_add3_u32 v1, v200, v181, s59\n"
    "v_add_u32 v2, 6144, v1\n"
    "v_add_u32 v3, 12288, v1\n"
    "v_add_u32 v4, 18432, v1\n"
    "v_mfma_f32_32x32x16_bf16 v[160:175], a[228:231], a[160:163], v[160:175]\n"
    "ds_read_b64_tr_b16 a[246:247], v18 offset:61952\n"
    "s_waitcnt lgkmcnt(4)\n"
    "v_mfma_f32_32x32x16_bf16 v[112:127], a[232:235], a[52:55], v[112:127]\n"
    "ds_read_b64_tr_b16 a[248:249], v18 offset:55296\n"
    "buffer_load_dwordx4 v6, s[40:43], s58 offen offset:1024 lds\n"
    "v_mfma_f32_32x32x16_bf16 v[128:143], a[232:235], a[164:167], v[128:143]\n"
    "ds_read_b64_tr_b16 a[250:251], v18 offset:55808\n"
    "v_mfma_f32_32x32x16_bf16 v[144:159], a[236:239], a[52:55], v[144:159]\n"
    "ds_read_b64_tr_b16 a[252:253], v18 offset:63488\n"
    "v_mfma_f32_32x32x16_bf16 v[160:175], a[236:239], a[164:167], v[160:175]\n"
    "ds_read_b64_tr_b16 a[254:255], v18 offset:64000\n"
    "s_waitcnt lgkmcnt(4)\n"
    "v_mfma_f32_32x32x16_bf16 v[112:127], a[240:243], a[56:59], v[112:127]\n"
    "ds_read_b64_tr_b16 a[224:225], v19 offset:0\n"
    "buffer_load_dwordx4 v7, s[40:43], s58 offen offset:2048 lds\n"
    "v_mfma_f32_32x32x16_bf16 v[128:143], a[240:243], a[168:171], v[128:143]\n"
    "ds_read_b64_tr_b16 a[226:227], v19 offset:512\n"
    "v_mfma_f32_32x32x16_bf16 v[144:159], a[244:247], a[56:59], v[144:159]\n"
    "ds_read_b64_tr_b16 a[228:229], v19 offset:8192\n"
    "v_mfma_f32_32x32x16_bf16 v[160:175], a[244:247], a[168:171], v[160:175]\n"
    "ds_read_b64_tr_b16 a[230:231], v19 offset:8704\n"
    "s_waitcnt lgkmcnt(4)\n"
    "v_mfma_f32_32x32x16_bf16 v[112:127], a[248:251], a[60:63], v[112:127]\n"
    "ds_read_b64_tr_b16 a[232:233], v19 offset:2048\n"
    "buffer_load_dwordx4 v8, s[40:43], s58 offen offset:3072 lds\n"
    "v_mfma_f32_32x32x16_bf16 v[128:143], a[248:251], a[172:175], v[128:143]\n"
    "ds_read_b64_tr_b16 a[234:235], v19 offset:2560\n"
    "v_mfma_f32_32x32x16_bf16 v[144:159], a[252:255], a[60:63], v[144:159]\n"
    "ds_read_b64_tr_b16 a[236:237], v19 offset:10240\n"
    "v_mfma_f32_32x32x16_bf16 v[160:175], a[252:255], a[172:175], v[160:175]\n"
    "ds_read_b64_tr_b16 a[238:239], v19 offset:10752\n"
    "s_waitcnt lgkmcnt(4)\n"
    "v_mfma_f32_32x32x16_bf16 v[112:127], a[224:227], a[64:67], v[112:127]\n"
    "ds_read_b64_tr_b16 a[240:241], v19 offset:4096\n"
    "s_add_u32 m0, s56, 0x8000\n"
    "s_mov_b32 s58, 0x70080\n"
    "s_nop 0\n"
    "buffer_load_dwordx4 v5, s[40:43], s58 offen offset:0 lds\n"
    "v_mfma_f32_32x32x16_bf16 v[128:143], a[224:227], a[176:179], v[128:143]\n"
    "ds_read_b64_tr_b16 a[242:243], v19 offset:4608\n"
    "v_mfma_f32_32x32x16_bf16 v[144:159], a[228:231], a[64:67], v[144:159]\n"
    "ds_read_b64_tr_b16 a[244:245], v19 offset:12288\n"
    "v_mfma_f32_32x32x16_bf16 v[160:175], a[228:231], a[176:179], v[160:175]\n"
    "ds_read_b64_tr_b16 a[246:247], v19 offset:12800\n"
    "s_waitcnt lgkmcnt(4)\n"
    "v_mfma_f32_32x32x16_bf16 v[112:127], a[232:235], a[68:71], v[112:127]\n"
    "ds_read_b64_tr_b16 a[248:249], v19 offset:6144\n"
    "buffer_load_dwordx4 v6, s[40:43], s58 offen offset:1024 lds\n"
    "v_mfma_f32_32x32x16_bf16 v[128:143], a[232:235], a[180:183], v[128:143]\n"
    "ds_read_b64_tr_b16 a[250:251], v19 offset:6656\n"
    "v_mfma_f32_32x32x16_bf16 v[144:159], a[236:239], a[68:71], v[144:159]\n"
    "ds_read_b64_tr_b16 a[252:253], v19 offset:14336\n"
    "v_mfma_f32_32x32x16_bf16 v[160:175], a[236:239], a[180:183], v[160:175]\n"
    "ds_read_b64_tr_b16 a[254:255], v19 offset:14848\n"
    "s_waitcnt vmcnt(12)\n"
    "s_barrier\n"
    "s_waitcnt lgkmcnt(4)\n"
    "v_mfma_f32_32x32x16_bf16 v[112:127], a[240:243], a[72:75], v[112:127]\n"
    "ds_read_b64_tr_b16 a[224:225], v18 offset:0\n"
    "buffer_load_dwordx4 v[252:255], v65, s[48:51], 0 offen offset:0 sc1\n"
    "buffer_load_dwordx4 v[92:95], v65, s[48:51], 0 offen offset:1024 sc1\n"
    "v_mfma_f32_32x32x16_bf16 v[128:143], a[240:243], a[184:187], v[128:143]\n"
    "ds_read_b64_tr_b16 a[226:227], v18 offset:512\n"
    "s_waitcnt vmcnt(9)\n"
    "s_mov_b32 s57, 655360\n"
    "buffer_store_dwordx2 v[46:47], v16, s[48:51], s57 offen offset:0 sc1\n"
    "s_and_b32 s33, s67, 1\n"
    "s_lshr_b32 s34, s67, 1\n"
    "s_lshl_b32 s35, s68, 1\n"
    "s_add_u32 s35, s35, s70\n"
    "s_lshl_b32 s35, s35, 1\n"
    "s_add_u32 s35, s35, s33\n"
    "s_lshl_b32 s59, s33, 5\n"
    "s_lshl_b32 s60, s71, 6\n"
    "s_add_u32 s59, s59, s60\n"
    "v_mfma_f32_32x32x16_bf16 v[144:159], a[244:247], a[72:75], v[144:159]\n"
    "ds_read_b64_tr_b16 a[228:229], v18 offset:8192\n"
    "v_add_u32 v26, s59, v20\n"
    "s_lshl_b32 s60, s34, 3\n"
    "v_add_u32 v27, s60, v21\n"
    "v_mul_u32_u24 v27, v27, v26\n"
    "v_cvt_f32_u32 v24, v26\n"
    "v_mfma_f32_32x32x16_bf16 v[160:175], a[244:247], a[184:187], v[160:175]\n"
    "ds_read_b64_tr_b16 a[230:231], v18 offset:8704\n"
    "v_cvt_f32_u32 v25, v27\n"
    "v_mul_f32 v24, 0x3b124925, v24\n"
    "v_mul_f32 v27, 0x3d124925, v25\n"
    "v_rndne_f32 v27, v27\n"
    "s_waitcnt lgkmcnt(4)\n"
    "v_mfma_f32_32x32x16_bf16 v[112:127], a[248:251], a[76:79], v[112:127]\n"
    "ds_read_b64_tr_b16 a[232:233], v18 offset:2048\n"
    "buffer_load_dwordx4 v7, s[40:43], s58 offen offset:2048 lds\n"
    "v_cos_f32 v40, v24\n"
    "v_fmamk_f32 v25, v27, 0xc1e00000, v25\n"
    "v_mfma_f32_32x32x16_bf16 v[128:143], a[248:251], a[188:191], v[128:143]\n"
    "ds_read_b64_tr_b16 a[234:235], v18 offset:2560\n"
    "v_add_f32 v40, v40, v40\n"
    "v_mul_f32 v25, 0x3d124925, v25\n"
    "v_sub_f32 v41, 0, v40\n"
    "v_lshlrev_b32 v27, 31, v20\n"
    "v_mfma_f32_32x32x16_bf16 v[144:159], a[252:255], a[76:79], v[144:159]\n"
    "ds_read_b64_tr_b16 a[236:237], v18 offset:10240\n"
    "v_xor_b32 v45, 0xbf800000, v27\n"
    "v_lshl_add_u32 v27, s34, 1, v21\n"
    "v_cmp_eq_u32 s[30:31], 0, v27\n"
    "v_cmp_eq_u32 s[96:97], 2, v27\n"
    "v_mfma_f32_32x32x16_bf16 v[160:175], a[252:255], a[188:191], v[160:175]\n"
    "ds_read_b64_tr_b16 a[238:239], v18 offset:10752\n"
    "v_lshl_add_u32 v28, v20, 6, s35\n"
    "v_cvt_f32_u32 v28, v28\n"
    "v_cvt_f32_u32 v27, v21\n"
    "v_mul_f32 v26, 0x3a000000, v28\n"
    "s_waitcnt lgkmcnt(4)\n"
    "v_mfma_f32_32x32x16_bf16 v[112:127], a[224:227], a[80:83], v[112:127]\n"
    "ds_read_b64_tr_b16 a[240:241], v18 offset:4096\n"
    "buffer_load_dwordx4 v8, s[40:43], s58 offen offset:3072 lds\n"
    "v_mul_f32 v27, 0x41000000, v27\n"
    "v_mul_f32 v27, v26, v27\n"
    "v_fmamk_f32 v28, v26, 0x00000000, v27\n"
    "v_mfma_f32_32x32x16_bf16 v[128:143], a[224:227], a[192:195], v[128:143]\n"
    "ds_read_b64_tr_b16 a[242:243], v18 offset:4608\n"
    "v_fmamk_f32 v29, v26, 0x3f800000, v27\n"
    "v_cos_f32 v30, v28\n"
    "v_mfma_f32_32x32x16_bf16 v[144:159], a[228:231], a[80:83], v[144:159]\n"
    "ds_read_b64_tr_b16 a[244:245], v18 offset:12288\n"
    "v_cos_f32 v31, v29\n"
    "v_sin_f32 v28, v28\n"
    "v_mfma_f32_32x32x16_bf16 v[160:175], a[228:231], a[192:195], v[160:175]\n"
    "ds_read_b64_tr_b16 a[246:247], v18 offset:12800\n"
    "v_sin_f32 v29, v29\n"
    "v_cvt_pk_bf16_f32 v32, v30, v31\n"
    "v_cvt_pk_bf16_f32 v48, v28, v29\n"
    "s_waitcnt lgkmcnt(4)\n"
    "v_mfma_f32_32x32x16_bf16 v[112:127], a[232:235], a[84:87], v[112:127]\n"
    "ds_read_b64_tr_b16 a[248:249], v18 offset:6144\n"
    "s_add_u32 m0, s56, 0x18000\n"
    "s_mov_b32 s58, 0xa8080\n"
    "s_nop 0\n"
    "buffer_load_dwordx4 v5, s[40:43], s58 offen offset:0 lds\n"
    "v_xor_b32 v56, 0x80008000, v48\n"
    "v_fmamk_f32 v28, v26, 0x40000000, v27\n"
    "v_mfma_f32_32x32x16_bf16 v[128:143], a[232:235], a[196:199], v[128:143]\n"
    "ds_read_b64_tr_b16 a[250:251], v18 offset:6656\n"
    "v_fmamk_f32 v29, v26, 0x40400000, v27\n"
    "v_cos_f32 v30, v28\n"
    "v_mfma_f32_32x32x16_bf16 v[144:159], a[236:239], a[84:87], v[144:159]\n"
    "ds_read_b64_tr_b16 a[252:253], v18 offset:14336\n"
    "v_cos_f32 v31, v29\n"
    "v_sin_f32 v28, v28\n"
    "v_mfma_f32_32x32x16_bf16 v[160:175], a[236:239], a[196:199], v[160:175]\n"
    "ds_read_b64_tr_b16 a[254:255], v18 offset:14848\n"
    "v_sin_f32 v29, v29\n"
    "v_cvt_pk_bf16_f32 v33, v30, v31\n"
    "v_cvt_pk_bf16_f32 v49, v28, v29\n"
    "s_waitcnt lgkmcnt(4)\n"
    "v_mfma_f32_32x32x16_bf16 v[112:127], a[240:243], a[88:91], v[112:127]\n"
    "ds_read_b64_tr_b16 a[224:225], v17 offset:0\n"
    "buffer_load_dwordx4 v6, s[40:43], s58 offen offset:1024 lds\n"
    "v_xor_b32 v57, 0x80008000, v49\n"
    "v_fmamk_f32 v28, v26, 0x40800000, v27\n"
    "v_mfma_f32_32x32x16_bf16 v[128:143], a[240:243], a[200:203], v[128:143]\n"
    "ds_read_b64_tr_b16 a[226:227], v17 offset:512\n"
    "v_fmamk_f32 v29, v26, 0x40a00000, v27\n"
    "v_cos_f32 v30, v28\n"
    "v_mfma_f32_32x32x16_bf16 v[144:159], a[244:247], a[88:91], v[144:159]\n"
    "ds_read_b64_tr_b16 a[228:229], v17 offset:8192\n"
    "v_cos_f32 v31, v29\n"
    "v_sin_f32 v28, v28\n"
    "v_mfma_f32_32x32x16_bf16 v[160:175], a[244:247], a[200:203], v[160:175]\n"
    "ds_read_b64_tr_b16 a[230:231], v17 offset:8704\n"
    "v_sin_f32 v29, v29\n"
    "v_cvt_pk_bf16_f32 v34, v30, v31\n"
    "v_cvt_pk_bf16_f32 v50, v28, v29\n"
    "s_waitcnt lgkmcnt(4)\n"
    "v_mfma_f32_32x32x16_bf16 v[112:127], a[248:251], a[92:95], v[112:127]\n"
    "ds_read_b64_tr_b16 a[232:233], v17 offset:2048\n"
    "buffer_load_dwordx4 v7, s[40:43], s58 offen offset:2048 lds\n"
    "v_xor_b32 v58, 0x80008000, v50\n"
    "v_fmamk_f32 v28, v26, 0x40c00000, v27\n"
    "v_mfma_f32_32x32x16_bf16 v[128:143], a[248:251], a[204:207], v[128:143]\n"
    "ds_read_b64_tr_b16 a[234:235], v17 offset:2560\n"
    "v_fmamk_f32 v29, v26, 0x40e00000, v27\n"
    "v_cos_f32 v30, v28\n"
    "v_mfma_f32_32x32x16_bf16 v[144:159], a[252:255], a[92:95], v[144:159]\n"
    "ds_read_b64_tr_b16 a[236:237], v17 offset:10240\n"
    "v_cos_f32 v31, v29\n"
    "v_sin_f32 v28, v28\n"
    "v_mfma_f32_32x32x16_bf16 v[160:175], a[252:255], a[204:207], v[160:175]\n"
    "ds_read_b64_tr_b16 a[238:239], v17 offset:10752\n"
    "v_sin_f32 v29, v29\n"
    "v_cvt_pk_bf16_f32 v35, v30, v31\n"
    "v_cvt_pk_bf16_f32 v51, v28, v29\n"
    "s_waitcnt lgkmcnt(4)\n"
    "v_mfma_f32_32x32x16_bf16 v[112:127], a[224:227], a[96:99], v[112:127]\n"
    "ds_read_b64_tr_b16 a[240:241], v17 offset:4096\n"
    "buffer_load_dwordx4 v8, s[40:43], s58 offen offset:3072 lds\n"
    "v_xor_b32 v59, 0x80008000, v51\n"
    "v_fmamk_f32 v28, v26, 0x41800000, v27\n"
    "v_mfma_f32_32x32x16_bf16 v[128:143], a[224:227], a[208:211], v[128:143]\n"
    "ds_read_b64_tr_b16 a[242:243], v17 offset:4608\n"
    "v_fmamk_f32 v29, v26, 0x41880000, v27\n"
    "v_cos_f32 v30, v28\n"
    "v_mfma_f32_32x32x16_bf16 v[144:159], a[228:231], a[96:99], v[144:159]\n"
    "ds_read_b64_tr_b16 a[244:245], v17 offset:12288\n"
    "v_cos_f32 v31, v29\n"
    "v_sin_f32 v28, v28\n"
    "v_mfma_f32_32x32x16_bf16 v[160:175], a[228:231], a[208:211], v[160:175]\n"
    "ds_read_b64_tr_b16 a[246:247], v17 offset:12800\n"
    "v_sin_f32 v29, v29\n"
    "v_cvt_pk_bf16_f32 v36, v30, v31\n"
    "v_cvt_pk_bf16_f32 v52, v28, v29\n"
    "s_waitcnt lgkmcnt(4)\n"
    "v_mfma_f32_32x32x16_bf16 v[112:127], a[232:235], a[100:103], v[112:127]\n"
    "ds_read_b64_tr_b16 a[248:249], v17 offset:6144\n"
    "s_add_u32 m0, s56, 0x1c000\n"
    "s_mov_b32 s58, 0xe0080\n"
    "s_nop 0\n"
    "buffer_load_dwordx4 v5, s[40:43], s58 offen offset:0 lds\n"
    "v_xor_b32 v60, 0x80008000, v52\n"
    "v_fmamk_f32 v28, v26, 0x41900000, v27\n"
    "v_mfma_f32_32x32x16_bf16 v[128:143], a[232:235], a[212:215], v[128:143]\n"
    "ds_read_b64_tr_b16 a[250:251], v17 offset:6656\n"
    "s_waitcnt vmcnt(8)\n"
    "v_cmp_ne_u32 vcc, s62, v253\n"
    "v_cmp_ne_u32 s[64:65], s63, v255\n"
    "s_nop 3\n"
    "s_or_b64 vcc, vcc, s[64:65]\n"
    "v_cmp_ne_u32 s[64:65], s62, v93\n"
    "s_nop 3\n"
    "s_or_b64 vcc, vcc, s[64:65]\n"
    "v_cmp_ne_u32 s[64:65], s63, v95\n"
    "s_nop 3\n"
    "s_or_b64 vcc, vcc, s[64:65]\n"
    "v_mfma_f32_32x32x16_bf16 v[144:159], a[236:239], a[100:103], v[144:159]\n"
    "ds_read_b64_tr_b16 a[252:253], v17 offset:14336\n"
    "s_cbranch_vccnz .Lm_cs_slow\n"
    ".Lm_cs_ok:\n"
    "v_mov_b32 v253, 0\n"
    "v_mov_b32 v255, 0\n"
    "v_mov_b32 v93, 0\n"
    "v_mov_b32 v95, 0\n"
    "v_mfma_f32_32x32x16_bf16 v[160:175], a[236:239], a[212:215], v[160:175]\n"
    "ds_read_b64_tr_b16 a[254:255], v17 offset:14848\n"
    "s_barrier\n"
    "buffer_load_dwordx4 v6, s[40:43], s58 offen offset:1024 lds\n"
    "s_waitcnt lgkmcnt(4)\n"
    "v_mfma_f32_32x32x16_bf16 v[112:127], a[240:243], a[104:107], v[112:127]\n"
    "ds_read_b64_tr_b16 a[224:225], v18 offset:16384\n"
    "v_fmamk_f32 v29, v26, 0x41980000, v27\n"
    "v_cos_f32 v30, v28\n"
    "v_mfma_f32_32x32x16_bf16 v[128:143], a[240:243], a[216:219], v[128:143]\n"
    "ds_read_b64_tr_b16 a[226:227], v18 offset:16896\n"
    "v_cos_f32 v31, v29\n"
    "v_sin_f32 v28, v28\n"
    "v_mfma_f32_32x32x16_bf16 v[144:159], a[244:247], a[104:107], v[144:159]\n"
    "ds_read_b64_tr_b16 a[228:229], v18 offset:24576\n"
    "v_sin_f32 v29, v29\n"
    "v_cvt_pk_bf16_f32 v37, v30, v31\n"
    "v_cvt_pk_bf16_f32 v53, v28, v29\n"
    "v_mfma_f32_32x32x16_bf16 v[160:175], a[244:247], a[216:219], v[160:175]\n"
    "ds_read_b64_tr_b16 a[230:231], v18 offset:25088\n"
    "v_xor_b32 v61, 0x80008000, v53\n"
    "buffer_load_dwordx4 v7, s[40:43], s58 offen offset:2048 lds\n"
    "v_fmamk_f32 v28, v26, 0x41a00000, v27\n"
    "s_waitcnt lgkmcnt(4)\n"
    "v_mfma_f32_32x32x16_bf16 v[112:127], a[248:251], a[108:111], v[112:127]\n"
    "ds_read_b64_tr_b16 a[232:233], v18 offset:18432\n"
    "v_fmamk_f32 v29, v26, 0x41a80000, v27\n"
    "v_cos_f32 v30, v28\n"
    "v_mfma_f32_32x32x16_bf16 v[128:143], a[248:251], a[220:223], v[128:143]\n"
    "ds_read_b64_tr_b16 a[234:235], v18 offset:18944\n"
    "v_cos_f32 v31, v29\n"
    "v_sin_f32 v28, v28\n"
    "v_mfma_f32_32x32x16_bf16 v[144:159], a[252:255], a[108:111], v[144:159]\n"
    "ds_read_b64_tr_b16 a[236:237], v18 offset:26624\n"
    "v_sin_f32 v29, v29\n"
    "v_cvt_pk_bf16_f32 v38, v30, v31\n"
    "v_cvt_pk_bf16_f32 v54, v28, v29\n"
    "v_mfma_f32_32x32x16_bf16 v[160:175], a[252:255], a[220:223], v[160:175]\n"
    "ds_read_b64_tr_b16 a[238:239], v18 offset:27136\n"
    "buffer_load_dwordx4 v8, s[40:43], s58 offen offset:3072 lds\n"
    "v_xor_b32 v62, 0x80008000, v54\n"
    "v_fmamk_f32 v28, v26, 0x41b00000, v27\n"
    "v_mfma_f32_32x32x16_bf16 v[112:127], v[252:255], v[242:245], v[112:127]\n"
    "v_mfma_f32_32x32x16_bf16 v[128:143], v[252:255], v[248:251], v[128:143]\n"
    "v_mfma_f32_32x32x16_bf16 v[144:159], v[252:255], v[240:243], v[144:159]\n"
    "v_mfma_f32_32x32x16_bf16 v[160:175], v[252:255], v[246:249], v[160:175]\n"
    "s_waitcnt lgkmcnt(4)\n"
    "v_mfma_f32_32x32x16_bf16 v[176:191], a[224:227], a[0:3], 0\n"
    "ds_read_b64_tr_b16 a[240:241], v18 offset:20480\n"
    "s_add_u32 m0, s56, 0xc000\n"
    "s_mul_i32 s58, s71, 114688\n"
    "s_nop 0\n"
    "buffer_load_dwordx4 v1, s[44:47], s58 offen offset:0 lds\n"
    "v_fmamk_f32 v29, v26, 0x41b80000, v27\n"
    "v_mfma_f32_32x32x16_bf16 v[192:207], a[224:227], a[112:115], 0\n"
    "ds_read_b64_tr_b16 a[242:243], v18 offset:20992\n"
    "s_nop 6\n"
    "v_mul_f32 v68, v72, v112\n"
    "v_mul_f32 v69, v72, v113\n"
    "v_mul_f32 v70, v72, v114\n"
    "v_mul_f32 v71, v72, v115\n"
    "v_mfma_f32_32x32x16_bf16 v[208:223], a[228:231], a[0:3], 0\n"
    "ds_read_b64_tr_b16 a[244:245], v18 offset:28672\n"
    "v_exp_f32 v68, v68\n"
    "v_exp_f32 v69, v69\n"
    "v_mfma_f32_32x32x16_bf16 v[224:239], a[228:231], a[112:115], 0\n"
    "ds_read_b64_tr_b16 a[246:247], v18 offset:29184\n"
    "v_exp_f32 v70, v70\n"
    "v_exp_f32 v71, v71\n"
    "s_waitcnt lgkmcnt(4)\n"
    "v_mfma_f32_32x32x16_bf16 v[176:191], a[232:235], a[4:7], v[176:191]\n"
    "ds_read_b64_tr_b16 a[248:249], v18 offset:22528\n"
    "buffer_load_dwordx4 v2, s[44:47], s58 offen offset:1024 lds\n"
    "v_mul_f32 v112, v112, v144\n"
    "v_mul_f32 v113, v113, v145\n"
    "v_mul_f32 v114, v114, v146\n"
    "v_mfma_f32_32x32x16_bf16 v[192:207], a[232:235], a[116:119], v[192:207]\n"
    "ds_read_b64_tr_b16 a[250:251], v18 offset:23040\n"
    "v_mul_f32 v115, v115, v147\n"
    "v_fma_f32 v68, v68, v74, v74\n"
    "v_fma_f32 v69, v69, v74, v74\n"
    "v_fma_f32 v70, v70, v74, v74\n"
    "v_mfma_f32_32x32x16_bf16 v[208:223], a[236:239], a[4:7], v[208:223]\n"
    "ds_read_b64_tr_b16 a[252:253], v18 offset:30720\n"
    "v_fma_f32 v71, v71, v74, v74\n"
    "v_rcp_f32 v68, v68\n"
    "v_mfma_f32_32x32x16_bf16 v[224:239], a[236:239], a[116:119], v[224:239]\n"
    "ds_read_b64_tr_b16 a[254:255], v18 offset:31232\n"
    "v_rcp_f32 v69, v69\n"
    "v_rcp_f32 v70, v70\n"
    "s_waitcnt lgkmcnt(4)\n"
    "v_mfma_f32_32x32x16_bf16 v[176:191], a[240:243], a[8:11], v[176:191]\n"
    "ds_read_b64_tr_b16 a[224:225], v17 offset:16384\n"
    "buffer_load_dwordx4 v3, s[44:47], s58 offen offset:2048 lds\n"
    "v_rcp_f32 v71, v71\n"
    "v_mul_f32 v112, v112, v68\n"
    "v_mfma_f32_32x32x16_bf16 v[192:207], a[240:243], a[120:123], v[192:207]\n"
    "ds_read_b64_tr_b16 a[226:227], v17 offset:16896\n"
    "v_mul_f32 v113, v113, v69\n"
    "v_mul_f32 v114, v114, v70\n"
    "v_mul_f32 v115, v115, v71\n"
    "v_cvt_pk_bf16_f32 v144, v112, v113\n"
    "v_mfma_f32_32x32x16_bf16 v[208:223], a[244:247], a[8:11], v[208:223]\n"
    "ds_read_b64_tr_b16 a[228:229], v17 offset:24576\n"
    "v_cvt_pk_bf16_f32 v145, v114, v115\n"
    "v_mul_f32 v68, v72, v116\n"
    "v_mul_f32 v69, v72, v117\n"
    "v_mul_f32 v70, v72, v118\n"
    "v_mfma_f32_32x32x16_bf16 v[224:239], a[244:247], a[120:123], v[224:239]\n"
    "ds_read_b64_tr_b16 a[230:231], v17 offset:25088\n"
    "v_mul_f32 v71, v72, v119\n"
    "v_exp_f32 v68, v68\n"
    "s_waitcnt lgkmcnt(4)\n"
    "v_mfma_f32_32x32x16_bf16 v[176:191], a[248:251], a[12:15], v[176:191]\n"
    "ds_read_b64_tr_b16 a[232:233], v17 offset:18432\n"
    "buffer_load_dwordx4 v4, s[44:47], s58 offen offset:3072 lds\n"
    "v_exp_f32 v69, v69\n"
    "v_mfma_f32_32x32x16_bf16 v[192:207], a[248:251], a[124:127], v[192:207]\n"
    "ds_read_b64_tr_b16 a[234:235], v17 offset:18944\n"
    "v_exp_f32 v70, v70\n"
    "v_exp_f32 v71, v71\n"
    "v_mul_f32 v116, v116, v148\n"
    "v_mfma_f32_32x32x16_bf16 v[208:223], a[252:255], a[12:15], v[208:223]\n"
    "ds_read_b64_tr_b16 a[236:237], v17 offset:26624\n"
    "v_mul_f32 v117, v117, v149\n"
    "v_mul_f32 v118, v118, v150\n"
    "v_mul_f32 v119, v119, v151\n"
    "v_fma_f32 v68, v68, v74, v74\n"
    "v_mfma_f32_32x32x16_bf16 v[224:239], a[252:255], a[124:127], v[224:239]\n"
    "ds_read_b64_tr_b16 a[238:239], v17 offset:27136\n"
    "v_fma_f32 v69, v69, v74, v74\n"
    "v_fma_f32 v70, v70, v74, v74\n"
    "v_fma_f32 v71, v71, v74, v74\n"
    "s_waitcnt lgkmcnt(4)\n"
    "v_mfma_f32_32x32x16_bf16 v[176:191], a[224:227], a[16:19], v[176:191]\n"
    "ds_read_b64_tr_b16 a[240:241], v17 offset:20480\n"
    "s_add_u32 m0, s56, 0x10000\n"
    "s_mov_b32 s58, 0x118080\n"
    "s_nop 0\n"
    "buffer_load_dwordx4 v5, s[40:43], s58 offen offset:0 lds\n"
    "v_rcp_f32 v68, v68\n"
    "v_mfma_f32_32x32x16_bf16 v[192:207], a[224:227], a[128:131], v[192:207]\n"
    "ds_read_b64_tr_b16 a[242:243], v17 offset:20992\n"
    "v_rcp_f32 v69, v69\n"
    "v_rcp_f32 v70, v70\n"
    "v_mfma_f32_32x32x16_bf16 v[208:223], a[228:231], a[16:19], v[208:223]\n"
    "ds_read_b64_tr_b16 a[244:245], v17 offset:28672\n"
    "v_rcp_f32 v71, v71\n"
    "v_mul_f32 v116, v116, v68\n"
    "v_mul_f32 v117, v117, v69\n"
    "v_mul_f32 v118, v118, v70\n"
    "v_mfma_f32_32x32x16_bf16 v[224:239], a[228:231], a[128:131], v[224:239]\n"
    "ds_read_b64_tr_b16 a[246:247], v17 offset:29184\n"
    "v_mul_f32 v119, v119, v71\n"
    "v_cvt_pk_bf16_f32 v146, v116, v117\n"
    "v_cvt_pk_bf16_f32 v147, v118, v119\n"
    "s_waitcnt lgkmcnt(4)\n"
    "v_mfma_f32_32x32x16_bf16 v[176:191], a[232:235], a[20:23], v[176:191]\n"
    "ds_read_b64_tr_b16 a[248:249], v17 offset:22528\n"
    "buffer_load_dwordx4 v6, s[40:43], s58 offen offset:1024 lds\n"
    "ds_write_b128 v13, v[144:147] offset:0\n"
    "v_mfma_f32_32x32x16_bf16 v[192:207], a[232:235], a[132:135], v[192:207]\n"
    "ds_read_b64_tr_b16 a[250:251], v17 offset:23040\n"
    "v_mul_f32 v68, v72, v120\n"
    "v_mul_f32 v69, v72, v121\n"
    "v_mul_f32 v70, v72, v122\n"
    "v_mul_f32 v71, v72, v123\n"
    "v_mfma_f32_32x32x16_bf16 v[208:223], a[236:239], a[20:23], v[208:223]\n"
    "ds_read_b64_tr_b16 a[252:253], v17 offset:30720\n"
    "v_exp_f32 v68, v68\n"
    "v_exp_f32 v69, v69\n"
    "v_mfma_f32_32x32x16_bf16 v[224:239], a[236:239], a[132:135], v[224:239]\n"
    "ds_read_b64_tr_b16 a[254:255], v17 offset:31232\n"
    "s_waitcnt vmcnt(10)\n"
    "s_barrier\n"
    "buffer_load_dwordx4 v7, s[40:43], s58 offen offset:2048 lds\n"
    "s_waitcnt lgkmcnt(5)\n"
    "v_mfma_f32_32x32x16_bf16 v[176:191], a[240:243], a[24:27], v[176:191]\n"
    "ds_read_b64_tr_b16 a[224:225], v17 offset:32768\n"
    "v_exp_f32 v70, v70\n"
    "v_exp_f32 v71, v71\n"
    "v_mul_f32 v120, v120, v152\n"
    "v_mfma_f32_32x32x16_bf16 v[192:207], a[240:243], a[136:139], v[192:207]\n"
    "ds_read_b64_tr_b16 a[226:227], v17 offset:33280\n"
    "v_mul_f32 v121, v121, v153\n"
    "v_mul_f32 v122, v122, v154\n"
    "v_mul_f32 v123, v123, v155\n"
    "v_fma_f32 v68, v68, v74, v74\n"
    "v_mfma_f32_32x32x16_bf16 v[208:223], a[244:247], a[24:27], v[208:223]\n"
    "ds_read_b64_tr_b16 a[228:229], v17 offset:40960\n"
    "v_fma_f32 v69, v69, v74, v74\n"
    "v_fma_f32 v70, v70, v74, v74\n"
    "v_fma_f32 v71, v71, v74, v74\n"
    "v_mfma_f32_32x32x16_bf16 v[224:239], a[244:247], a[136:139], v[224:239]\n"
    "ds_read_b64_tr_b16 a[230:231], v17 offset:41472\n"
    "v_rcp_f32 v68, v68\n"
    "buffer_load_dwordx4 v8, s[40:43], s58 offen offset:3072 lds\n"
    "s_waitcnt lgkmcnt(4)\n"
    "v_mfma_f32_32x32x16_bf16 v[176:191], a[248:251], a[28:31], v[176:191]\n"
    "ds_read_b64_tr_b16 a[232:233], v17 offset:34816\n"
    "v_rcp_f32 v69, v69\n"
    "v_rcp_f32 v70, v70\n"
    "v_mfma_f32_32x32x16_bf16 v[192:207], a[248:251], a[140:143], v[192:207]\n"
    "ds_read_b64_tr_b16 a[234:235], v17 offset:35328\n"
    "v_rcp_f32 v71, v71\n"
    "v_mul_f32 v120, v120, v68\n"
    "v_mul_f32 v121, v121, v69\n"
    "v_mul_f32 v122, v122, v70\n"
    "v_mfma_f32_32x32x16_bf16 v[208:223], a[252:255], a[28:31], v[208:223]\n"
    "ds_read_b64_tr_b16 a[236:237], v17 offset:43008\n"
    "v_mul_f32 v123, v123, v71\n"
    "v_cvt_pk_bf16_f32 v148, v120, v121\n"
    "v_cvt_pk_bf16_f32 v149, v122, v123\n"
    "v_mul_f32 v68, v72, v124\n"
    "v_mfma_f32_32x32x16_bf16 v[224:239], a[252:255], a[140:143], v[224:239]\n"
    "ds_read_b64_tr_b16 a[238:239], v17 offset:43520\n"
    "s_add_u32 m0, s56, 0x20000\n"
    "s_mov_b32 s58, 0x150080\n"
    "s_nop 0\n"
    "buffer_load_dwordx4 v5, s[40:43], s58 offen offset:0 lds\n"
    "v_mul_f32 v69, v72, v125\n"
    "v_mul_f32 v70, v72, v126\n"
    "s_waitcnt lgkmcnt(4)\n"
    "v_mfma_f32_32x32x16_bf16 v[176:191], a[224:227], a[32:35], v[176:191]\n"
    "ds_read_b64_tr_b16 a[240:241], v17 offset:36864\n"
    "v_mul_f32 v71, v72, v127\n"
    "v_exp_f32 v68, v68\n"
    "v_exp_f32 v69, v69\n"
    "v_mfma_f32_32x32x16_bf16 v[192:207], a[224:227], a[144:147], v[192:207]\n"
    "ds_read_b64_tr_b16 a[242:243], v17 offset:37376\n"
    "v_exp_f32 v70, v70\n"
    "v_exp_f32 v71, v71\n"
    "v_mul_f32 v124, v124, v156\n"
    "v_mfma_f32_32x32x16_bf16 v[208:223], a[228:231], a[32:35], v[208:223]\n"
    "ds_read_b64_tr_b16 a[244:245], v17 offset:45056\n"
    "v_mul_f32 v125, v125, v157\n"
    "v_mul_f32 v126, v126, v158\n"
    "v_mul_f32 v127, v127, v159\n"
    "v_fma_f32 v68, v68, v74, v74\n"
    "v_mfma_f32_32x32x16_bf16 v[224:239], a[228:231], a[144:147], v[224:239]\n"
    "ds_read_b64_tr_b16 a[246:247], v17 offset:45568\n"
    "buffer_load_dwordx4 v6, s[40:43], s58 offen offset:1024 lds\n"
    "v_fma_f32 v69, v69, v74, v74\n"
    "v_fma_f32 v70, v70, v74, v74\n"
    "s_waitcnt lgkmcnt(4)\n"
    "v_mfma_f32_32x32x16_bf16 v[176:191], a[232:235], a[36:39], v[176:191]\n"
    "ds_read_b64_tr_b16 a[248:249], v17 offset:38912\n"
    "v_fma_f32 v71, v71, v74, v74\n"
    "v_rcp_f32 v68, v68\n"
    "v_mfma_f32_32x32x16_bf16 v[192:207], a[232:235], a[148:151], v[192:207]\n"
    "ds_read_b64_tr_b16 a[250:251], v17 offset:39424\n"
    "v_rcp_f32 v69, v69\n"
    "v_rcp_f32 v70, v70\n"
    "v_mfma_f32_32x32x16_bf16 v[208:223], a[236:239], a[36:39], v[208:223]\n"
    "ds_read_b64_tr_b16 a[252:253], v17 offset:47104\n"
    "v_rcp_f32 v71, v71\n"
    "v_mul_f32 v124, v124, v68\n"
    "v_mul_f32 v125, v125, v69\n"
    "v_mul_f32 v126, v126, v70\n"
    "v_mfma_f32_32x32x16_bf16 v[224:239], a[236:239], a[148:151], v[224:239]\n"
    "ds_read_b64_tr_b16 a[254:255], v17 offset:47616\n"
    "buffer_load_dwordx4 v7, s[40:43], s58 offen offset:2048 lds\n"
    "v_mul_f32 v127, v127, v71\n"
    "v_cvt_pk_bf16_f32 v150, v124, v125\n"
    "s_waitcnt lgkmcnt(4)\n"
    "v_mfma_f32_32x32x16_bf16 v[176:191], a[240:243], a[40:43], v[176:191]\n"
    "ds_read_b64_tr_b16 a[224:225], v18 offset:32768\n"
    "v_cvt_pk_bf16_f32 v151, v126, v127\n"
    "ds_write_b128 v13, v[148:151] offset:1024\n"
    "v_mfma_f32_32x32x16_bf16 v[192:207], a[240:243], a[152:155], v[192:207]\n"
    "ds_read_b64_tr_b16 a[226:227], v18 offset:33280\n"
    "v_mul_f32 v68, v73, v128\n"
    "v_mul_f32 v69, v73, v129\n"
    "v_mul_f32 v70, v73, v130\n"
    "v_mul_f32 v71, v73, v131\n"
    "v_mfma_f32_32x32x16_bf16 v[208:223], a[244:247], a[40:43], v[208:223]\n"
    "ds_read_b64_tr_b16 a[228:229], v18 offset:40960\n"
    "v_exp_f32 v68, v68\n"
    "v_exp_f32 v69, v69\n"
    "v_mfma_f32_32x32x16_bf16 v[224:239], a[244:247], a[152:155], v[224:239]\n"
    "ds_read_b64_tr_b16 a[230:231], v18 offset:41472\n"
    "buffer_load_dwordx4 v8, s[40:43], s58 offen offset:3072 lds\n"
    "v_exp_f32 v70, v70\n"
    "s_waitcnt lgkmcnt(5)\n"
    "v_mfma_f32_32x32x16_bf16 v[176:191], a[248:251], a[44:47], v[176:191]\n"
    "ds_read_b64_tr_b16 a[232:233], v18 offset:34816\n"
    "v_exp_f32 v71, v71\n"
    "v_mul_f32 v128, v128, v160\n"
    "v_mul_f32 v129, v129, v161\n"
    "v_mul_f32 v130, v130, v162\n"
    "v_mfma_f32_32x32x16_bf16 v[192:207], a[248:251], a[156:159], v[192:207]\n"
    "ds_read_b64_tr_b16 a[234:235], v18 offset:35328\n"
    "v_mul_f32 v131, v131, v163\n"
    "v_fma_f32 v68, v68, v75, v75\n"
    "v_fma_f32 v69, v69, v75, v75\n"
    "v_fma_f32 v70, v70, v75, v75\n"
    "v_mfma_f32_32x32x16_bf16 v[208:223], a[252:255], a[44:47], v[208:223]\n"
    "ds_read_b64_tr_b16 a[236:237], v18 offset:43008\n"
    "v_fma_f32 v71, v71, v75, v75\n"
    "v_rcp_f32 v68, v68\n"
    "v_mfma_f32_32x32x16_bf16 v[224:239], a[252:255], a[156:159], v[224:239]\n"
    "ds_read_b64_tr_b16 a[238:239], v18 offset:43520\n"
    "s_add_u32 m0, s56, 0x0\n"
    "s_cmp_le_u32 s71, 0\ns_addc_u32 s58, 0, 0\n"
    "s_mul_i32 s58, s58, 114688\n"
    "s_nop 0\n"
    "buffer_load_dwordx4 v1, s[44:47], s58 offen offset:0 lds\n"
    "s_waitcnt lgkmcnt(4)\n"
    "v_mfma_f32_32x32x16_bf16 v[176:191], a[224:227], a[48:51], v[176:191]\n"
    "ds_read_b64_tr_b16 a[240:241], v18 offset:36864\n"
    "v_rcp_f32 v69, v69\n"
    "v_rcp_f32 v70, v70\n"
    "v_mfma_f32_32x32x16_bf16 v[192:207], a[224:227], a[160:163], v[192:207]\n"
    "ds_read_b64_tr_b16 a[242:243], v18 offset:37376\n"
    "v_rcp_f32 v71, v71\n"
    "v_mul_f32 v128, v128, v68\n"
    "v_mul_f32 v129, v129, v69\n"
    "v_mul_f32 v130, v130, v70\n"
    "v_mfma_f32_32x32x16_bf16 v[208:223], a[228:231], a[48:51], v[208:223]\n"
    "ds_read_b64_tr_b16 a[244:245], v18 offset:45056\n"
    "v_mul_f32 v131, v131, v71\n"
    "v_cvt_pk_bf16_f32 v160, v128, v129\n"
    "v_cvt_pk_bf16_f32 v161, v130, v131\n"
    "v_mul_f32 v68, v73, v132\n"
    "v_mfma_f32_32x32x16_bf16 v[224:239], a[228:231], a[160:163], v[224:239]\n"
    "ds_read_b64_tr_b16 a[246:247], v18 offset:45568\n"
    "buffer_load_dwordx4 v2, s[44:47], s58 offen offset:1024 lds\n"
    "v_mul_f32 v69, v73, v133\n"
    "v_mul_f32 v70, v73, v134\n"
    "v_mul_f32 v71, v73, v135\n"
    "s_waitcnt lgkmcnt(4)\n"
    "v_mfma_f32_32x32x16_bf16 v[176:191], a[232:235], a[52:55], v[176:191]\n"
    "ds_read_b64_tr_b16 a[248:249], v18 offset:38912\n"
    "v_exp_f32 v68, v68\n"
    "v_exp_f32 v69, v69\n"
    "v_mfma_f32_32x32x16_bf16 v[192:207], a[232:235], a[164:167], v[192:207]\n"
    "ds_read_b64_tr_b16 a[250:251], v18 offset:39424\n"
    "v_exp_f32 v70, v70\n"
    "v_exp_f32 v71, v71\n"
    "v_mul_f32 v132, v132, v164\n"
    "v_mfma_f32_32x32x16_bf16 v[208:223], a[236:239], a[52:55], v[208:223]\n"
    "ds_read_b64_tr_b16 a[252:253], v18 offset:47104\n"
    "v_mul_f32 v133, v133, v165\n"
    "v_mul_f32 v134, v134, v166\n"
    "v_mul_f32 v135, v135, v167\n"
    "v_fma_f32 v68, v68, v75, v75\n"
    "v_mfma_f32_32x32x16_bf16 v[224:239], a[236:239], a[164:167], v[224:239]\n"
    "ds_read_b64_tr_b16 a[254:255], v18 offset:47616\n"
    "buffer_load_dwordx4 v3, s[44:47], s58 offen offset:2048 lds\n"
    "s_waitcnt vmcnt(7)\n"
    "s_barrier\n"
    "s_waitcnt lgkmcnt(4)\n"
    "v_mfma_f32_32x32x16_bf16 v[176:191], a[240:243], a[56:59], v[176:191]\n"
    "ds_read_b64_tr_b16 a[224:225], v18 offset:49152\n"
    "v_fma_f32 v69, v69, v75, v75\n"
    "v_fma_f32 v70, v70, v75, v75\n"
    "v_fma_f32 v71, v71, v75, v75\n"
    "v_mfma_f32_32x32x16_bf16 v[192:207], a[240:243], a[168:171], v[192:207]\n"
    "ds_read_b64_tr_b16 a[226:227], v18 offset:49664\n"
    "buffer_load_dwordx4 v4, s[44:47], s58 offen offset:3072 lds\n"
    "v_rcp_f32 v68, v68\n"
    "v_mfma_f32_32x32x16_bf16 v[208:223], a[244:247], a[56:59], v[208:223]\n"
    "ds_read_b64_tr_b16 a[228:229], v18 offset:57344\n"
    "v_rcp_f32 v69, v69\n"
    "v_rcp_f32 v70, v70\n"
    "v_mfma_f32_32x32x16_bf16 v[224:239], a[244:247], a[168:171], v[224:239]\n"
    "ds_read_b64_tr_b16 a[230:231], v18 offset:57856\n"
    "v_rcp_f32 v71, v71\n"
    "v_mul_f32 v132, v132, v68\n"
    "v_mul_f32 v133, v133, v69\n"
    "s_waitcnt lgkmcnt(4)\n"
    "v_mfma_f32_32x32x16_bf16 v[176:191], a[248:251], a[60:63], v[176:191]\n"
    "ds_read_b64_tr_b16 a[232:233], v18 offset:51200\n"
    "v_mul_f32 v134, v134, v70\n"
    "v_mul_f32 v135, v135, v71\n"
    "v_cvt_pk_bf16_f32 v162, v132, v133\n"
    "v_cvt_pk_bf16_f32 v163, v134, v135\n"
    "v_mfma_f32_32x32x16_bf16 v[192:207], a[248:251], a[172:175], v[192:207]\n"
    "ds_read_b64_tr_b16 a[234:235], v18 offset:51712\n"
    "s_add_u32 m0, s56, 0x14000\n"
    "s_cmp_le_u32 s71, 1\ns_addc_u32 s58, 1, 0\n"
    "s_mul_i32 s58, s58, 114688\n"
    "s_nop 0\n"
    "buffer_load_dwordx4 v1, s[44:47], s58 offen offset:0 lds\n"
    "v_xor_b32 v13, 4096, v13\n"
    "v_mfma_f32_32x32x16_bf16 v[208:223], a[252:255], a[60:63], v[208:223]\n"
    "ds_read_b64_tr_b16 a[236:237], v18 offset:59392\n"
    "ds_write_b128 v13, v[160:163] offset:0\n"
    "v_mul_f32 v68, v73, v136\n"
    "v_mfma_f32_32x32x16_bf16 v[224:239], a[252:255], a[172:175], v[224:239]\n"
    "ds_read_b64_tr_b16 a[238:239], v18 offset:59904\n"
    "v_mul_f32 v69, v73, v137\n"
    "v_mul_f32 v70, v73, v138\n"
    "v_mul_f32 v71, v73, v139\n"
    "s_waitcnt lgkmcnt(5)\n"
    "v_mfma_f32_32x32x16_bf16 v[176:191], a[224:227], a[64:67], v[176:191]\n"
    "ds_read_b64_tr_b16 a[240:241], v18 offset:53248\n"
    "v_exp_f32 v68, v68\n"
    "v_exp_f32 v69, v69\n"
    "v_mfma_f32_32x32x16_bf16 v[192:207], a[224:227], a[176:179], v[192:207]\n"
    "ds_read_b64_tr_b16 a[242:243], v18 offset:53760\n"
    "buffer_load_dwordx4 v2, s[44:47], s58 offen offset:1024 lds\n"
    "v_exp_f32 v70, v70\n"
    "v_mfma_f32_32x32x16_bf16 v[208:223], a[228:231], a[64:67], v[208:223]\n"
    "ds_read_b64_tr_b16 a[244:245], v18 offset:61440\n"
    "v_exp_f32 v71, v71\n"
    "v_mul_f32 v136, v136, v168\n"
    "v_mul_f32 v137, v137, v169\n"
    "v_mul_f32 v138, v138, v170\n"
    "v_mfma_f32_32x32x16_bf16 v[224:239], a[228:231], a[176:179], v[224:239]\n"
    "ds_read_b64_tr_b16 a[246:247], v18 offset:61952\n"
    "v_mul_f32 v139, v139, v171\n"
    "v_fma_f32 v68, v68, v75, v75\n"
    "v_fma_f32 v69, v69, v75, v75\n"
    "v_fma_f32 v70, v70, v75, v75\n"
    "s_waitcnt lgkmcnt(4)\n"
    "v_mfma_f32_32x32x16_bf16 v[176:191], a[232:235], a[68:71], v[176:191]\n"
    "ds_read_b64_tr_b16 a[248:249], v18 offset:55296\n"
    "v_fma_f32 v71, v71, v75, v75\n"
    "v_rcp_f32 v68, v68\n"
    "v_mfma_f32_32x32x16_bf16 v[192:207], a[232:235], a[180:183], v[192:207]\n"
    "ds_read_b64_tr_b16 a[250:251], v18 offset:55808\n"
    "buffer_load_dwordx4 v3, s[44:47], s58 offen offset:2048 lds\n"
    "v_rcp_f32 v69, v69\n"
    "v_mfma_f32_32x32x16_bf16 v[208:223], a[236:239], a[68:71], v[208:223]\n"
    "ds_read_b64_tr_b16 a[252:253], v18 offset:63488\n"
    "v_rcp_f32 v70, v70\n"
    "v_rcp_f32 v71, v71\n"
    "v_mul_f32 v136, v136, v68\n"
    "v_mfma_f32_32x32x16_bf16 v[224:239], a[236:239], a[180:183], v[224:239]\n"
    "ds_read_b64_tr_b16 a[254:255], v18 offset:64000\n"
    "v_mul_f32 v137, v137, v69\n"
    "v_mul_f32 v138, v138, v70\n"
    "v_mul_f32 v139, v139, v71\n"
    "v_cvt_pk_bf16_f32 v164, v136, v137\n"
    "s_waitcnt lgkmcnt(4)\n"
    "v_mfma_f32_32x32x16_bf16 v[176:191], a[240:243], a[72:75], v[176:191]\n"
    "ds_read_b64_tr_b16 a[224:225], v18 offset:0\n"
    "v_cvt_pk_bf16_f32 v165, v138, v139\n"
    "v_mul_f32 v68, v73, v140\n"
    "v_mul_f32 v69, v73, v141\n"
    "v_mul_f32 v70, v73, v142\n"
    "v_mfma_f32_32x32x16_bf16 v[192:207], a[240:243], a[184:187], v[192:207]\n"
    "ds_read_b64_tr_b16 a[226:227], v18 offset:512\n"
    "buffer_load_dwordx4 v4, s[44:47], s58 offen offset:3072 lds\n"
    "v_mul_f32 v71, v73, v143\n"
    "v_exp_f32 v68, v68\n"
    "v_mfma_f32_32x32x16_bf16 v[208:223], a[244:247], a[72:75], v[208:223]\n"
    "ds_read_b64_tr_b16 a[228:229], v18 offset:8192\n"
    "v_exp_f32 v69, v69\n"
    "v_exp_f32 v70, v70\n"
    "v_mfma_f32_32x32x16_bf16 v[224:239], a[244:247], a[184:187], v[224:239]\n"
    "ds_read_b64_tr_b16 a[230:231], v18 offset:8704\n"
    "v_exp_f32 v71, v71\n"
    "v_mul_f32 v140, v140, v172\n"
    "v_mul_f32 v141, v141, v173\n"
    "v_mul_f32 v142, v142, v174\n"
    "v_mul_f32 v143, v143, v175\n"
    "v_fma_f32 v68, v68, v75, v75\n"
    "v_fma_f32 v69, v69, v75, v75\n"
    "v_fma_f32 v70, v70, v75, v75\n"
    "v_fma_f32 v71, v71, v75, v75\n"
    "v_rcp_f32 v68, v68\n"
    "s_add_u32 m0, s56, 0x4000\n"
    "s_cmp_le_u32 s71, 2\ns_addc_u32 s58, 2, 0\n"
    "s_mul_i32 s58, s58, 114688\n"
    "s_nop 0\n"
    "buffer_load_dwordx4 v1, s[44:47], s58 offen offset:0 lds\n"
    "v_rcp_f32 v69, v69\n"
    "v_rcp_f32 v70, v70\n"
    "v_rcp_f32 v71, v71\n"
    "v_mul_f32 v140, v140, v68\n"
    "v_mul_f32 v141, v141, v69\n"
    "v_mul_f32 v142, v142, v70\n"
    "v_mul_f32 v143, v143, v71\n"
    "v_cvt_pk_bf16_f32 v166, v140, v141\n"
    "v_cvt_pk_bf16_f32 v167, v142, v143\n"
    "ds_write_b128 v13, v[164:167] offset:1024\n"
    "buffer_store_dwordx4 v[144:147], v9, s[48:51], 0 offen offset:0 sc1\n"
    "buffer_store_dwordx4 v[148:151], v9, s[48:51], 0 offen offset:1024 sc1\n"
    "buffer_store_dwordx4 v[160:163], v64, s[48:51], 0 offen offset:0 sc1\n"
    "buffer_load_dwordx4 v2, s[44:47], s58 offen offset:1024 lds\n"
    "buffer_store_dwordx4 v[164:167], v64, s[48:51], 0 offen offset:1024 sc1\n"
    "s_waitcnt lgkmcnt(5)\n"
    "v_mfma_f32_32x32x16_bf16 v[176:191], a[248:251], a[76:79], v[176:191]\n"
    "ds_read_b64_tr_b16 a[232:233], v18 offset:2048\n"
    "v_cos_f32 v30, v28\n"
    "v_cos_f32 v31, v29\n"
    "v_mfma_f32_32x32x16_bf16 v[192:207], a[248:251], a[188:191], v[192:207]\n"
    "ds_read_b64_tr_b16 a[234:235], v18 offset:2560\n"
    "v_sin_f32 v28, v28\n"
    "v_sin_f32 v29, v29\n"
    "v_mfma_f32_32x32x16_bf16 v[208:223], a[252:255], a[76:79], v[208:223]\n"
    "ds_read_b64_tr_b16 a[236:237], v18 offset:10240\n"
    "v_cvt_pk_bf16_f32 v39, v30, v31\n"
    "v_cvt_pk_bf16_f32 v55, v28, v29\n"
    "v_xor_b32 v63, 0x80008000, v55\n"
    "s_lshl_b32 s59, s70, 1\n"
    "s_and_b32 s60, s67, 1\n"
    "s_add_u32 s59, s59, s60\n"
    "s_lshl_b32 s60, s68, 2\n"
    "s_add_u32 s59, s59, s60\n"
    "v_mfma_f32_32x32x16_bf16 v[224:239], a[252:255], a[188:191], v[224:239]\n"
    "ds_read_b64_tr_b16 a[238:239], v18 offset:10752\n"
    "buffer_load_dwordx4 v3, s[44:47], s58 offen offset:2048 lds\n"
    "s_mul_i32 s60, s70, 56\n"
    "s_and_b32 s61, s67, 2\n"
    "s_add_u32 s60, s60, s61\n"
    "s_mul_i32 s61, s59, 112\n"
    "s_add_u32 s61, s61, s60\n"
    "s_lshl_b32 s61, s61, 10\n"
    "s_add_u32 s61, s61, 6291456\n"
    "s_waitcnt lgkmcnt(5)\n"
    "v_mfma_f32_32x32x16_bf16 v[176:191], a[224:227], a[80:83], v[176:191]\n"
    "ds_read_b64_tr_b16 a[240:241], v18 offset:4096\n"
    "v_lshl_add_u32 v10, v0, 4, s61\n"
    "s_xor_b32 s59, s59, 2\n"
    "s_mul_i32 s61, s59, 112\n"
    "s_add_u32 s61, s61, s60\n"
    "s_lshl_b32 s61, s61, 10\n"
    "s_add_u32 s61, s61, 6291456\n"
    "v_lshl_add_u32 v11, v0, 4, s61\n"
    "s_lshl_b32 s59, s68, 1\n"
    "s_add_u32 s59, s59, s70\n"
    "v_mfma_f32_32x32x16_bf16 v[192:207], a[224:227], a[192:195], v[192:207]\n"
    "ds_read_b64_tr_b16 a[242:243], v18 offset:4608\n"
    "s_mul_i32 s59, s59, 3584\n"
    "s_lshr_b32 s60, s67, 1\n"
    "s_lshl_b32 s60, s60, 7\n"
    "s_add_u32 s59, s59, s60\n"
    "s_add_u32 s59, s59, 524288\n"
    "v_min_u32 v14, 13, v0\n"
    "v_lshl_add_u32 v14, v14, 8, s59\n"
    "v_mfma_f32_32x32x16_bf16 v[208:223], a[228:231], a[80:83], v[208:223]\n"
    "ds_read_b64_tr_b16 a[244:245], v18 offset:12288\n"
    "v_lshl_add_u32 v15, v20, 2, v21\n"
    "s_and_b32 s59, s67, 2\n"
    "v_add_lshl_u32 v15, v15, s59, 7\n"
    "v_add_u32 v15, 4096, v15\n"
    "v_mfma_f32_32x32x16_bf16 v[224:239], a[228:231], a[192:195], v[224:239]\n"
    "ds_read_b64_tr_b16 a[246:247], v18 offset:12800\n"
    "buffer_load_dwordx4 v4, s[44:47], s58 offen offset:3072 lds\n"
    "v_fmamk_f32 v96, v24, 0x00000000, v25\n"
    "v_fmamk_f32 v97, v24, 0x40800000, v25\n"
    "s_waitcnt lgkmcnt(4)\n"
    "v_mfma_f32_32x32x16_bf16 v[176:191], a[232:235], a[84:87], v[176:191]\n"
    "ds_read_b64_tr_b16 a[248:249], v18 offset:6144\n"
    "v_fmamk_f32 v98, v24, 0x41000000, v25\n"
    "v_fmamk_f32 v99, v24, 0x41400000, v25\n"
    "v_fmamk_f32 v100, v24, 0x42000000, v25\n"
    "v_fmamk_f32 v101, v24, 0x42100000, v25\n"
    "v_mfma_f32_32x32x16_bf16 v[192:207], a[232:235], a[196:199], v[192:207]\n"
    "ds_read_b64_tr_b16 a[250:251], v18 offset:6656\n"
    "v_fmamk_f32 v102, v24, 0x42200000, v25\n"
    "v_fmamk_f32 v103, v24, 0x42300000, v25\n"
    "v_fmamk_f32 v104, v24, 0x42800000, v25\n"
    "v_fmamk_f32 v105, v24, 0x42880000, v25\n"
    "v_mfma_f32_32x32x16_bf16 v[208:223], a[236:239], a[84:87], v[208:223]\n"
    "ds_read_b64_tr_b16 a[252:253], v18 offset:14336\n"
    "v_fmamk_f32 v106, v24, 0x42900000, v25\n"
    "v_fmamk_f32 v107, v24, 0x42980000, v25\n"
    "v_fmamk_f32 v108, v24, 0x42c00000, v25\n"
    "v_fmamk_f32 v109, v24, 0x42c80000, v25\n"
    "v_mfma_f32_32x32x16_bf16 v[224:239], a[236:239], a[196:199], v[224:239]\n"
    "ds_read_b64_tr_b16 a[254:255], v18 offset:14848\n"
    "s_add_u32 m0, s56, 0x8000\n"
    "s_cmp_le_u32 s71, 3\ns_addc_u32 s58, 3, 0\n"
    "s_mul_i32 s58, s58, 114688\n"
    "s_nop 0\n"
    "buffer_load_dwordx4 v1, s[44:47], s58 offen offset:0 lds\n"
    "s_waitcnt vmcnt(17)\n"
    "s_barrier\n"
    "s_waitcnt lgkmcnt(4)\n"
    "v_mfma_f32_32x32x16_bf16 v[176:191], a[240:243], a[88:91], v[176:191]\n"
    "ds_read_b64_tr_b16 a[224:225], v19 offset:0\n"
    "v_fmamk_f32 v110, v24, 0x42d00000, v25\n"
    "v_fmamk_f32 v111, v24, 0x42d80000, v25\n"
    "v_cos_f32 v96, v96\n"
    "v_mfma_f32_32x32x16_bf16 v[192:207], a[240:243], a[200:203], v[192:207]\n"
    "ds_read_b64_tr_b16 a[226:227], v19 offset:512\n"
    "buffer_load_dwordx4 v2, s[44:47], s58 offen offset:1024 lds\n"
    "v_cos_f32 v97, v97\n"
    "v_mfma_f32_32x32x16_bf16 v[208:223], a[244:247], a[88:91], v[208:223]\n"
    "ds_read_b64_tr_b16 a[228:229], v19 offset:8192\n"
    "v_cos_f32 v98, v98\n"
    "v_cos_f32 v99, v99\n"
    "v_mfma_f32_32x32x16_bf16 v[224:239], a[244:247], a[200:203], v[224:239]\n"
    "ds_read_b64_tr_b16 a[230:231], v19 offset:8704\n"
    "v_cos_f32 v100, v100\n"
    "v_cos_f32 v101, v101\n"
    "s_waitcnt lgkmcnt(4)\n"
    "v_mfma_f32_32x32x16_bf16 v[176:191], a[248:251], a[92:95], v[176:191]\n"
    "ds_read_b64_tr_b16 a[232:233], v19 offset:2048\n"
    "v_cos_f32 v102, v102\n"
    "v_cos_f32 v103, v103\n"
    "v_mfma_f32_32x32x16_bf16 v[192:207], a[248:251], a[204:207], v[192:207]\n"
    "ds_read_b64_tr_b16 a[234:235], v19 offset:2560\n"
    "buffer_load_dwordx4 v3, s[44:47], s58 offen offset:2048 lds\n"
    "v_cos_f32 v104, v104\n"
    "v_mfma_f32_32x32x16_bf16 v[208:223], a[252:255], a[92:95], v[208:223]\n"
    "ds_read_b64_tr_b16 a[236:237], v19 offset:10240\n"
    "v_cos_f32 v105, v105\n"
    "v_cos_f32 v106, v106\n"
    "v_mfma_f32_32x32x16_bf16 v[224:239], a[252:255], a[204:207], v[224:239]\n"
    "ds_read_b64_tr_b16 a[238:239], v19 offset:10752\n"
    "v_cos_f32 v107, v107\n"
    "v_cos_f32 v108, v108\n"
    "s_waitcnt lgkmcnt(4)\n"
    "v_mfma_f32_32x32x16_bf16 v[176:191], a[224:227], a[96:99], v[176:191]\n"
    "ds_read_b64_tr_b16 a[240:241], v19 offset:4096\n"
    "v_cos_f32 v109, v109\n"
    "v_cos_f32 v110, v110\n"
    "v_mfma_f32_32x32x16_bf16 v[192:207], a[224:227], a[208:211], v[192:207]\n"
    "ds_read_b64_tr_b16 a[242:243], v19 offset:4608\n"
    "buffer_load_dwordx4 v4, s[44:47], s58 offen offset:3072 lds\n"
    "v_cos_f32 v111, v111\n"
    "v_fmamk_f32 v76, v24, 0x3f800000, v25\n"
    "v_mfma_f32_32x32x16_bf16 v[208:223], a[228:231], a[96:99], v[208:223]\n"
    "ds_read_b64_tr_b16 a[244:245], v19 offset:12288\n"
    "v_fmamk_f32 v77, v24, 0x40a00000, v25\n"
    "v_fmamk_f32 v78, v24, 0x41100000, v25\n"
    "v_fmamk_f32 v79, v24, 0x41500000, v25\n"
    "v_fmamk_f32 v80, v24, 0x42040000, v25\n"
    "v_mfma_f32_32x32x16_bf16 v[224:239], a[228:231], a[208:211], v[224:239]\n"
    "ds_read_b64_tr_b16 a[246:247], v19 offset:12800\n"
    "v_fmamk_f32 v81, v24, 0x42140000, v25\n"
    "v_fmamk_f32 v82, v24, 0x42240000, v25\n"
    "v_fmamk_f32 v83, v24, 0x42340000, v25\n"
    "s_waitcnt lgkmcnt(4)\n"
    "v_mfma_f32_32x32x16_bf16 v[176:191], a[232:235], a[100:103], v[176:191]\n"
    "ds_read_b64_tr_b16 a[248:249], v19 offset:6144\n"
    "v_fmamk_f32 v84, v24, 0x42820000, v25\n"
    "v_fmamk_f32 v85, v24, 0x428a0000, v25\n"
    "v_fmamk_f32 v86, v24, 0x42920000, v25\n"
    "v_fmamk_f32 v87, v24, 0x429a0000, v25\n"
    "v_mfma_f32_32x32x16_bf16 v[192:207], a[232:235], a[212:215], v[192:207]\n"
    "ds_read_b64_tr_b16 a[250:251], v19 offset:6656\n"
    "s_add_u32 m0, s56, 0x18000\n"
    "s_cmp_le_u32 s71, 4\ns_addc_u32 s58, 4, 0\n"
    "s_mul_i32 s58, s58, 114688\n"
    "s_nop 0\n"
    "buffer_load_dwordx4 v1, s[44:47], s58 offen offset:0 lds\n"
    "v_fmamk_f32 v88, v24, 0x42c20000, v25\n"
    "v_mfma_f32_32x32x16_bf16 v[208:223], a[236:239], a[100:103], v[208:223]\n"
    "ds_read_b64_tr_b16 a[252:253], v19 offset:14336\n"
    "v_fmamk_f32 v89, v24, 0x42ca0000, v25\n"
    "v_fmamk_f32 v90, v24, 0x42d20000, v25\n"
    "v_fmamk_f32 v91, v24, 0x42da0000, v25\n"
    "v_mfma_f32_32x32x16_bf16 v[224:239], a[236:239], a[212:215], v[224:239]\n"
    "ds_read_b64_tr_b16 a[254:255], v19 offset:14848\n"
    "v_cos_f32 v76, v76\n"
    "v_cos_f32 v77, v77\n"
    "s_waitcnt lgkmcnt(4)\n"
    "v_mfma_f32_32x32x16_bf16 v[176:191], a[240:243], a[104:107], v[176:191]\n"
    "v_cos_f32 v78, v78\n"
    "v_cos_f32 v79, v79\n"
    "v_cos_f32 v80, v80\n"
    "v_mfma_f32_32x32x16_bf16 v[192:207], a[240:243], a[216:219], v[192:207]\n"
    "v_cos_f32 v81, v81\n"
    "v_cos_f32 v82, v82\n"
    "v_cos_f32 v83, v83\n"
    "v_mfma_f32_32x32x16_bf16 v[208:223], a[244:247], a[104:107], v[208:223]\n"
    "v_cos_f32 v84, v84\n"
    "v_cos_f32 v85, v85\n"
    "v_cos_f32 v86, v86\n"
    "v_mfma_f32_32x32x16_bf16 v[224:239], a[244:247], a[216:219], v[224:239]\n"
    "v_cos_f32 v87, v87\n"
    "v_cos_f32 v88, v88\n"
    "s_waitcnt lgkmcnt(0)\n"
    "v_mfma_f32_32x32x16_bf16 v[176:191], a[248:251], a[108:111], v[176:191]\n"
    "v_cos_f32 v89, v89\n"
    "v_cos_f32 v90, v90\n"
    "v_cos_f32 v91, v91\n"
    "v_mfma_f32_32x32x16_bf16 v[192:207], a[248:251], a[220:223], v[192:207]\n"
    "v_mfma_f32_32x32x16_bf16 v[208:223], a[252:255], a[108:111], v[208:223]\n"
    "v_mfma_f32_32x32x16_bf16 v[224:239], a[252:255], a[220:223], v[224:239]\n"
    "s_waitcnt vmcnt(7)\n"
    "s_mov_b32 s57, 524288\n"
    "buffer_store_dwordx2 v[46:47], v16, s[48:51], s57 offen offset:0 sc1\n"
    "v_mfma_f32_32x32x16_bf16 v[176:191], v[92:95], v[242:245], v[176:191]\n"
    "v_mfma_f32_32x32x16_bf16 v[192:207], v[92:95], v[248:251], v[192:207]\n"
    "v_mfma_f32_32x32x16_bf16 v[208:223], v[92:95], v[240:243], v[208:223]\n"
    "v_mfma_f32_32x32x16_bf16 v[224:239], v[92:95], v[246:249], v[224:239]\n"
    "s_barrier\n"
    "s_lshl_b32 s59, s67, 12\n"
    "s_and_b32 s60, s67, 1\n"
    "s_lshl_b32 s60, s60, 11\n"
    "s_add_u32 s59, s59, s60\n"
    "s_add_u32 s59, s59, 131072\n"
    "v_lshl_add_u32 v17, v0, 4, s59\n"
    "buffer_load_dwordx4 v2, s[44:47], s58 offen offset:1024 lds\n"
    "s_nop 9\n"
    "v_pk_mul_f32 v[68:69], v[176:177], v[72:73] op_sel:[0,0] op_sel_hi:[1,0]\n"
    "v_pk_mul_f32 v[70:71], v[178:179], v[72:73] op_sel:[0,0] op_sel_hi:[1,0]\n"
    "v_exp_f32 v68, v68\n"
    "v_exp_f32 v69, v69\n"
    "v_exp_f32 v70, v70\n"
    "v_exp_f32 v71, v71\n"
    "v_pk_mul_f32 v[176:177], v[176:177], v[208:209]\n"
    "v_pk_mul_f32 v[178:179], v[178:179], v[210:211]\n"
    "v_pk_fma_f32 v[68:69], v[68:69], v[74:75], v[74:75] op_sel:[0,0,0] op_sel_hi:[1,0,0]\n"
    "v_pk_fma_f32 v[70:71], v[70:71], v[74:75], v[74:75] op_sel:[0,0,0] op_sel_hi:[1,0,0]\n"
    "v_rcp_f32 v68, v68\n"
    "v_rcp_f32 v69, v69\n"
    "v_rcp_f32 v70, v70\n"
    "v_rcp_f32 v71, v71\n"
    "v_pk_mul_f32 v[176:177], v[176:177], v[68:69]\n"
    "v_pk_mul_f32 v[178:179], v[178:179], v[70:71]\n"
    "v_cvt_pk_bf16_f32 v208, v176, v177\n"
    "v_cvt_pk_bf16_f32 v209, v178, v179\n"
    "v_pk_mul_f32 v[68:69], v[180:181], v[72:73] op_sel:[0,0] op_sel_hi:[1,0]\n"
    "v_pk_mul_f32 v[70:71], v[182:183], v[72:73] op_sel:[0,0] op_sel_hi:[1,0]\n"
    "v_exp_f32 v68, v68\n"
    "v_exp_f32 v69, v69\n"
    "v_exp_f32 v70, v70\n"
    "v_exp_f32 v71, v71\n"
    "v_pk_mul_f32 v[180:181], v[180:181], v[212:213]\n"
    "v_pk_mul_f32 v[182:183], v[182:183], v[214:215]\n"
    "v_pk_fma_f32 v[68:69], v[68:69], v[74:75], v[74:75] op_sel:[0,0,0] op_sel_hi:[1,0,0]\n"
    "v_pk_fma_f32 v[70:71], v[70:71], v[74:75], v[74:75] op_sel:[0,0,0] op_sel_hi:[1,0,0]\n"
    "v_rcp_f32 v68, v68\n"
    "v_rcp_f32 v69, v69\n"
    "v_rcp_f32 v70, v70\n"
    "v_rcp_f32 v71, v71\n"
    "v_pk_mul_f32 v[180:181], v[180:181], v[68:69]\n"
    "v_pk_mul_f32 v[182:183], v[182:183], v[70:71]\n"
    "v_cvt_pk_bf16_f32 v210, v180, v181\n"
    "v_cvt_pk_bf16_f32 v211, v182, v183\n"
    "ds_write_b128 v17, v[208:211] offset:0\n"
    "v_pk_mul_f32 v[68:69], v[184:185], v[72:73] op_sel:[0,0] op_sel_hi:[1,0]\n"
    "v_pk_mul_f32 v[70:71], v[186:187], v[72:73] op_sel:[0,0] op_sel_hi:[1,0]\n"
    "v_exp_f32 v68, v68\n"
    "buffer_load_dwordx4 v3, s[44:47], s58 offen offset:2048 lds\n"
    "v_exp_f32 v69, v69\n"
    "v_exp_f32 v70, v70\n"
    "v_exp_f32 v71, v71\n"
    "v_pk_mul_f32 v[184:185], v[184:185], v[216:217]\n"
    "v_pk_mul_f32 v[186:187], v[186:187], v[218:219]\n"
    "v_pk_fma_f32 v[68:69], v[68:69], v[74:75], v[74:75] op_sel:[0,0,0] op_sel_hi:[1,0,0]\n"
    "v_pk_fma_f32 v[70:71], v[70:71], v[74:75], v[74:75] op_sel:[0,0,0] op_sel_hi:[1,0,0]\n"
    "v_rcp_f32 v68, v68\n"
    "v_rcp_f32 v69, v69\n"
    "v_rcp_f32 v70, v70\n"
    "v_rcp_f32 v71, v71\n"
    "v_pk_mul_f32 v[184:185], v[184:185], v[68:69]\n"
    "v_pk_mul_f32 v[186:187], v[186:187], v[70:71]\n"
    "v_cvt_pk_bf16_f32 v212, v184, v185\n"
    "v_cvt_pk_bf16_f32 v213, v186, v187\n"
    "v_pk_mul_f32 v[68:69], v[188:189], v[72:73] op_sel:[0,0] op_sel_hi:[1,0]\n"
    "v_pk_mul_f32 v[70:71], v[190:191], v[72:73] op_sel:[0,0] op_sel_hi:[1,0]\n"
    "buffer_load_dwordx4 v4, s[44:47], s58 offen offset:3072 lds\n"
    "v_exp_f32 v68, v68\n"
    "v_exp_f32 v69, v69\n"
    "v_exp_f32 v70, v70\n"
    "v_exp_f32 v71, v71\n"
    "v_pk_mul_f32 v[188:189], v[188:189], v[220:221]\n"
    "v_pk_mul_f32 v[190:191], v[190:191], v[222:223]\n"
    "v_pk_fma_f32 v[68:69], v[68:69], v[74:75], v[74:75] op_sel:[0,0,0] op_sel_hi:[1,0,0]\n"
    "v_pk_fma_f32 v[70:71], v[70:71], v[74:75], v[74:75] op_sel:[0,0,0] op_sel_hi:[1,0,0]\n"
    "v_rcp_f32 v68, v68\n"
    "v_rcp_f32 v69, v69\n"
    "v_rcp_f32 v70, v70\n"
    "v_rcp_f32 v71, v71\n"
    "v_pk_mul_f32 v[188:189], v[188:189], v[68:69]\n"
    "v_pk_mul_f32 v[190:191], v[190:191], v[70:71]\n"
    "v_cvt_pk_bf16_f32 v214, v188, v189\n"
    "s_add_u32 m0, s56, 0x1c000\n"
    "s_cmp_le_u32 s71, 5\ns_addc_u32 s58, 5, 0\n"
    "s_mul_i32 s58, s58, 114688\n"
    "s_nop 0\n"
    "buffer_load_dwordx4 v1, s[44:47], s58 offen offset:0 lds\n"
    "v_cvt_pk_bf16_f32 v215, v190, v191\n"
    "ds_write_b128 v17, v[212:215] offset:1024\n"
    "v_pk_mul_f32 v[68:69], v[192:193], v[72:73] op_sel:[0,1] op_sel_hi:[1,1]\n"
    "v_pk_mul_f32 v[70:71], v[194:195], v[72:73] op_sel:[0,1] op_sel_hi:[1,1]\n"
    "v_exp_f32 v68, v68\n"
    "v_exp_f32 v69, v69\n"
    "v_exp_f32 v70, v70\n"
    "v_exp_f32 v71, v71\n"
    "v_pk_mul_f32 v[192:193], v[192:193], v[224:225]\n"
    "v_pk_mul_f32 v[194:195], v[194:195], v[226:227]\n"
    "v_pk_fma_f32 v[68:69], v[68:69], v[74:75], v[74:75] op_sel:[0,1,1] op_sel_hi:[1,1,1]\n"
    "v_pk_fma_f32 v[70:71], v[70:71], v[74:75], v[74:75] op_sel:[0,1,1] op_sel_hi:[1,1,1]\n"
    "v_rcp_f32 v68, v68\n"
    "v_rcp_f32 v69, v69\n"
    "v_rcp_f32 v70, v70\n"
    "buffer_load_dwordx4 v2, s[44:47], s58 offen offset:1024 lds\n"
    "v_rcp_f32 v71, v71\n"
    "v_pk_mul_f32 v[192:193], v[192:193], v[68:69]\n"
    "v_pk_mul_f32 v[194:195], v[194:195], v[70:71]\n"
    "v_cvt_pk_bf16_f32 v224, v192, v193\n"
    "v_cvt_pk_bf16_f32 v225, v194, v195\n"
    "v_pk_mul_f32 v[68:69], v[196:197], v[72:73] op_sel:[0,1] op_sel_hi:[1,1]\n"
    "v_pk_mul_f32 v[70:71], v[198:199], v[72:73] op_sel:[0,1] op_sel_hi:[1,1]\n"
    "v_exp_f32 v68, v68\n"
    "v_exp_f32 v69, v69\n"
    "v_exp_f32 v70, v70\n"
    "v_exp_f32 v71, v71\n"
    "v_pk_mul_f32 v[196:197], v[196:197], v[228:229]\n"
    "v_pk_mul_f32 v[198:199], v[198:199], v[230:231]\n"
    "v_pk_fma_f32 v[68:69], v[68:69], v[74:75], v[74:75] op_sel:[0,1,1] op_sel_hi:[1,1,1]\n"
    "v_pk_fma_f32 v[70:71], v[70:71], v[74:75], v[74:75] op_sel:[0,1,1] op_sel_hi:[1,1,1]\n"
    "v_rcp_f32 v68, v68\n"
    "v_rcp_f32 v69, v69\n"
    "buffer_load_dwordx4 v3, s[44:47], s58 offen offset:2048 lds\n"
    "v_rcp_f32 v70, v70\n"
    "v_rcp_f32 v71, v71\n"
    "v_pk_mul_f32 v[196:197], v[196:197], v[68:69]\n"
    "v_pk_mul_f32 v[198:199], v[198:199], v[70:71]\n"
    "v_cvt_pk_bf16_f32 v226, v196, v197\n"
    "v_cvt_pk_bf16_f32 v227, v198, v199\n"
    "buffer_load_dwordx4 v4, s[44:47], s58 offen offset:3072 lds\n"
    "buffer_load_dwordx2 v[22:23], v14, s[48:51], 0 offen offset:0 sc1\n"
    "v_xor_b32 v17, 4096, v17\n"
    "ds_write_b128 v17, v[224:227] offset:0\n"
    "v_pk_mul_f32 v[68:69], v[200:201], v[72:73] op_sel:[0,1] op_sel_hi:[1,1]\n"
    "v_pk_mul_f32 v[70:71], v[202:203], v[72:73] op_sel:[0,1] op_sel_hi:[1,1]\n"
    "v_exp_f32 v68, v68\n"
    "v_exp_f32 v69, v69\n"
    "v_exp_f32 v70, v70\n"
    "v_exp_f32 v71, v71\n"
    "v_pk_mul_f32 v[200:201], v[200:201], v[232:233]\n"
    "v_pk_mul_f32 v[202:203], v[202:203], v[234:235]\n"
    "v_pk_fma_f32 v[68:69], v[68:69], v[74:75], v[74:75] op_sel:[0,1,1] op_sel_hi:[1,1,1]\n"
    "v_pk_fma_f32 v[70:71], v[70:71], v[74:75], v[74:75] op_sel:[0,1,1] op_sel_hi:[1,1,1]\n"
    "v_rcp_f32 v68, v68\n"
    "v_rcp_f32 v69, v69\n"
    "v_rcp_f32 v70, v70\n"
    "v_rcp_f32 v71, v71\n"
    "v_pk_mul_f32 v[200:201], v[200:201], v[68:69]\n"
    "v_pk_mul_f32 v[202:203], v[202:203], v[70:71]\n"
    "v_cvt_pk_bf16_f32 v228, v200, v201\n"
    "v_cvt_pk_bf16_f32 v229, v202, v203\n"
    "v_pk_mul_f32 v[68:69], v[204:205], v[72:73] op_sel:[0,1] op_sel_hi:[1,1]\n"
    "v_pk_mul_f32 v[70:71], v[206:207], v[72:73] op_sel:[0,1] op_sel_hi:[1,1]\n"
    "v_exp_f32 v68, v68\n"
    "v_exp_f32 v69, v69\n"
    "v_exp_f32 v70, v70\n"
    "v_exp_f32 v71, v71\n"
    "v_pk_mul_f32 v[204:205], v[204:205], v[236:237]\n"
    "v_pk_mul_f32 v[206:207], v[206:207], v[238:239]\n"
    "v_pk_fma_f32 v[68:69], v[68:69], v[74:75], v[74:75] op_sel:[0,1,1] op_sel_hi:[1,1,1]\n"
    "v_pk_fma_f32 v[70:71], v[70:71], v[74:75], v[74:75] op_sel:[0,1,1] op_sel_hi:[1,1,1]\n"
    "v_rcp_f32 v68, v68\n"
    "v_rcp_f32 v69, v69\n"
    "v_rcp_f32 v70, v70\n"
    "v_rcp_f32 v71, v71\n"
    "v_pk_mul_f32 v[204:205], v[204:205], v[68:69]\n"
    "v_pk_mul_f32 v[206:207], v[206:207], v[70:71]\n"
    "v_cvt_pk_bf16_f32 v230, v204, v205\n"
    "v_cvt_pk_bf16_f32 v231, v206, v207\n"
    "ds_write_b128 v17, v[228:231] offset:1024\n"
    "buffer_store_dwordx4 v[208:211], v9, s[48:51], s73 offen offset:0 sc1\n"
    "buffer_store_dwordx4 v[212:215], v9, s[48:51], s73 offen offset:1024 sc1\n"
    "buffer_store_dwordx4 v[224:227], v64, s[48:51], s73 offen offset:0 sc1\n"
    "buffer_store_dwordx4 v[228:231], v64, s[48:51], s73 offen offset:1024 sc1\n"
    "s_lshl_b32 s59, s34, 12\n"
    "s_lshl_b32 s60, s34, 6\n"
    "v_bfe_u32 v12, v0, 2, 2\n"
    "v_lshl_add_u32 v12, v21, 2, v12\n"
    "v_lshlrev_b32 v12, 7, v12\n"
    "v_bfe_u32 v13, v0, 3, 1\n"
    "v_lshlrev_b32 v13, 6, v13\n"
    "v_xor_b32 v13, s60, v13\n"
    "v_add3_u32 v12, v12, v13, s59\n"
    "v_bfe_u32 v13, v0, 4, 1\n"
    "v_lshl_add_u32 v12, v13, 5, v12\n"
    "v_and_b32 v13, 3, v0\n"
    "v_lshl_add_u32 v5, v13, 3, v12\n"
    "v_xor_b32 v8, 64, v5\n"
    "v_add_u32 v6, 65536, v5\n"
    "v_add_u32 v7, 131072, v5\n"
    "v_add_u32 v194, 65536, v8\n"
    "v_add_u32 v18, 131072, v8\n"
    "s_lshl_b32 s59, s70, 1\n"
    "s_add_u32 s59, s59, s33\n"
    "s_lshl_b32 s59, s59, 1\n"
    "s_add_u32 s59, s59, s34\n"
    "s_lshl_b32 s59, s59, 11\n"
    "v_lshl_add_u32 v12, v0, 4, s59\n"
    "v_add_u32 v92, 147456, v12\n"
    "v_add_u32 v94, 131072, v12\n"
    "v_xor_b32 v93, 8192, v92\n"
    "v_xor_b32 v95, 8192, v94\n"
    "s_waitcnt vmcnt(18) lgkmcnt(0)\n"
    "s_barrier\n"
    "ds_read_b64_tr_b16 a[224:225], v5 offset:49152\n"
    "ds_read_b64_tr_b16 a[226:227], v5 offset:50176\n"
    "ds_read_b64_tr_b16 a[228:229], v8 offset:49152\n"
    "ds_read_b64_tr_b16 a[230:231], v8 offset:50176\n"
    "ds_read_b128 a[0:3], v93 offset:0\n"
    "ds_read_b128 a[4:7], v92 offset:0\n"
    "ds_read_b64_tr_b16 a[232:233], v5 offset:51200\n"
    "ds_read_b64_tr_b16 a[234:235], v5 offset:52224\n"
    "ds_read_b64_tr_b16 a[236:237], v8 offset:51200\n"
    "ds_read_b64_tr_b16 a[238:239], v8 offset:52224\n"
    "ds_read_b128 a[8:11], v93 offset:1024\n"
    "ds_read_b128 a[12:15], v92 offset:1024\n"
    "ds_read_b128 a[208:211], v95 offset:0\n"
    "ds_read_b128 a[212:215], v94 offset:0\n"
    "ds_read_b128 a[216:219], v95 offset:1024\n"
    "s_waitcnt lgkmcnt(13)\n"
    "ds_read_b128 a[220:223], v94 offset:1024\n"
    "s_mov_b32 s46, 0xffff0000\n"
    "v_lshlrev_b32 v12, 4, v0\n"
    "s_lshl_b32 s59, s33, 10\n"
    "s_lshl_b32 s60, s34, 10\n"
    "v_add_u32 v13, s59, v12\n"
    "v_add_u32 v12, s60, v12\n"
    "s_mov_b32 s59, 0x10000\n"
    "s_waitcnt lgkmcnt(11)\n"
    "v_mfma_f32_32x32x16_bf16 v[176:191], a[224:227], a[0:3], 0\n"
    "ds_read_b64_tr_b16 a[240:241], v5 offset:57344\n"
    "s_cmp_eq_u32 s34, 0\ns_cselect_b32 s59, s59, 0x20000\n"
    "v_add_u32 v27, s59, v12\n"
    "v_xor_b32 v30, 1024, v27\n"
    "v_add_u32 v26, s59, v13\n"
    "s_mov_b32 s59, 0x24000\n"
    "v_mfma_f32_32x32x16_bf16 v[208:223], a[228:231], a[0:3], 0\n"
    "ds_read_b64_tr_b16 a[242:243], v5 offset:58368\n"
    "s_cmp_eq_u32 s34, 0\ns_cselect_b32 s59, s59, 0x26000\n"
    "v_add_u32 v28, s59, v12\n"
    "v_xor_b32 v31, 1024, v28\n"
    "v_add_u32 v195, s59, v13\n"
    "s_mov_b32 s59, 0xc000\n"
    "s_waitcnt lgkmcnt(12)\n"
    "v_mfma_f32_32x32x16_bf16 a[112:127], a[224:227], a[4:7], 0\n"
    "ds_read_b64_tr_b16 a[244:245], v8 offset:57344\n"
    "s_cmp_eq_u32 s34, 0\ns_cselect_b32 s59, s59, 0xe000\n"
    "v_add_u32 v29, s59, v12\n"
    "v_xor_b32 v192, 1024, v29\n"
    "v_add_u32 v206, s59, v13\n"
    "s_mul_i32 s59, s35, 34048\n"
    "s_lshl_b32 s60, s34, 9\n"
    "s_add_u32 s59, s59, s60\n"
    "v_mfma_f32_32x32x16_bf16 a[128:143], a[228:231], a[4:7], 0\n"
    "ds_read_b64_tr_b16 a[246:247], v8 offset:58368\n"
    "s_add_u32 s59, s59, 3670016\n"
    "v_lshlrev_b32 v12, 13, v21\n"
    "v_lshl_add_u32 v12, v20, 4, v12\n"
    "v_add_u32 v19, s59, v12\n"
    "s_waitcnt lgkmcnt(9)\n"
    "v_mfma_f32_32x32x16_bf16 v[176:191], a[232:235], a[8:11], v[176:191]\n"
    "ds_read_b64_tr_b16 a[248:249], v5 offset:59392\n"
    "v_lshl_add_u32 v12, s34, 5, v20\n"
    "v_cmp_lt_u32 s[64:65], 56, v12\n"
    "v_mov_b32 v12, 0x7ffffff0\n"
    "s_nop 0\n"
    "v_cndmask_b32 v19, v19, v12, s[64:65]\n"
    "s_mov_b32 s75, 0x05040100\n"
    "v_mfma_f32_32x32x16_bf16 v[208:223], a[236:239], a[8:11], v[208:223]\n"
    "ds_read_b64_tr_b16 a[250:251], v5 offset:60416\n"
    "s_mov_b32 s40, 0x07060302\n"
    "s_waitcnt lgkmcnt(10)\n"
    "v_mfma_f32_32x32x16_bf16 a[112:127], a[232:235], a[12:15], a[112:127]\n"
    "ds_read_b64_tr_b16 a[252:253], v8 offset:59392\n"
    "v_mfma_f32_32x32x16_bf16 a[128:143], a[236:239], a[12:15], a[128:143]\n"
    "ds_read_b64_tr_b16 a[254:255], v8 offset:60416\n"
    "s_waitcnt vmcnt(4)\n"
    "v_cmp_ne_u32 vcc, s62, v22\n"
    "v_cmp_ne_u32 s[64:65], s63, v23\n"
    "s_nop 3\n"
    "s_or_b64 vcc, vcc, s[64:65]\n"
    "s_cbranch_vccnz .Lm_h0_slow\n"
    ".Lm_h0_ok:\n"
    "s_cmp_le_u32 s71, 0\ns_addc_u32 s58, 0, 0\n"
    "s_mul_i32 s58, s58, 8192\n"
    "buffer_load_dwordx4 a[16:19], v11, s[48:51], s58 offen offset:0 sc1\n"
    "s_waitcnt lgkmcnt(4)\n"
    "v_mfma_f32_32x32x16_bf16 v[176:191], a[240:243], a[208:211], v[176:191]\n"
    "ds_read_b64_tr_b16 a[224:225], v5 offset:57344\n"
    "ds_read_b64_tr_b16 a[226:227], v5 offset:58368\n"
    "buffer_load_dwordx4 a[20:23], v10, s[48:51], s58 offen offset:0 sc1\n"
    "buffer_load_dwordx4 a[24:27], v11, s[48:51], s58 offen offset:1024 sc1\n"
    "buffer_load_dwordx4 a[28:31], v10, s[48:51], s58 offen offset:1024 sc1\n"
    "v_mfma_f32_32x32x16_bf16 v[208:223], a[244:247], a[208:211], v[208:223]\n"
    "ds_read_b64_tr_b16 a[228:229], v8 offset:57344\n"
    "ds_read_b64_tr_b16 a[230:231], v8 offset:58368\n"
    "s_cmp_le_u32 s71, 1\ns_addc_u32 s58, 1, 0\n"
    "s_mul_i32 s58, s58, 8192\n"
    "buffer_load_dwordx4 a[32:35], v11, s[48:51], s58 offen offset:0 sc1\n"
    "buffer_load_dwordx4 a[36:39], v10, s[48:51], s58 offen offset:0 sc1\n"
    "buffer_load_dwordx4 a[40:43], v11, s[48:51], s58 offen offset:1024 sc1\n"
    "buffer_load_dwordx4 a[44:47], v10, s[48:51], s58 offen offset:1024 sc1\n"
    "s_waitcnt lgkmcnt(4)\n"
    "v_mfma_f32_32x32x16_bf16 v[176:191], a[248:251], a[216:219], v[176:191]\n"
    "ds_read_b64_tr_b16 a[232:233], v5 offset:59392\n"
    "ds_read_b64_tr_b16 a[234:235], v5 offset:60416\n"
    "v_mfma_f32_32x32x16_bf16 v[208:223], a[252:255], a[216:219], v[208:223]\n"
    "ds_read_b64_tr_b16 a[236:237], v8 offset:59392\n"
    "ds_read_b64_tr_b16 a[238:239], v8 offset:60416\n"
    "s_waitcnt lgkmcnt(4)\n"
    "v_mfma_f32_32x32x16_bf16 a[112:127], a[224:227], a[212:215], a[112:127]\n"
    "ds_read_b64_tr_b16 a[240:241], v5 offset:0\n"
    "ds_read_b64_tr_b16 a[242:243], v5 offset:1024\n"
    "v_mfma_f32_32x32x16_bf16 a[128:143], a[228:231], a[212:215], a[128:143]\n"
    "ds_read_b64_tr_b16 a[244:245], v8 offset:0\n"
    "ds_read_b64_tr_b16 a[246:247], v8 offset:1024\n"
    "s_waitcnt lgkmcnt(4)\n"
    "v_mfma_f32_32x32x16_bf16 a[112:127], a[232:235], a[220:223], a[112:127]\n"
    "ds_read_b64_tr_b16 a[248:249], v5 offset:2048\n"
    "ds_read_b64_tr_b16 a[250:251], v5 offset:3072\n"
    "v_mfma_f32_32x32x16_bf16 a[128:143], a[236:239], a[220:223], a[128:143]\n"
    "ds_read_b64_tr_b16 a[252:253], v8 offset:2048\n"
    "ds_read_b64_tr_b16 a[254:255], v8 offset:3072\n"
    "s_barrier\n"
    "v_cndmask_b32 v44, v96, 0, s[30:31]\n"
    "v_cvt_pk_bf16_f32 v160, v44, v97\n"
    "v_cvt_pk_bf16_f32 v161, v98, v99\n"
    "v_cvt_pk_bf16_f32 v162, v100, v101\n"
    "v_cvt_pk_bf16_f32 v163, v102, v103\n"
    "ds_write_b128 v26, v[160:163] offset:0\n"
    "v_cvt_pk_bf16_f32 v164, v104, v105\n"
    "v_cvt_pk_bf16_f32 v165, v106, v107\n"
    "v_cndmask_b32 v44, v108, 1.0, s[96:97]\n"
    "v_cvt_pk_bf16_f32 v166, v44, v109\n"
    "v_cvt_pk_bf16_f32 v167, v110, v111\n"
    "ds_write_b128 v26, v[164:167] offset:2048\n"
    "v_cvt_pk_bf16_f32 v160, v76, v77\n"
    "v_cvt_pk_bf16_f32 v161, v78, v79\n"
    "v_cvt_pk_bf16_f32 v162, v80, v81\n"
    "v_cvt_pk_bf16_f32 v163, v82, v83\n"
    "ds_write_b128 v26, v[160:163] offset:8192\n"
    "v_cvt_pk_bf16_f32 v164, v84, v85\n"
    "v_cvt_pk_bf16_f32 v165, v86, v87\n"
    "v_cndmask_b32 v44, v88, 0, s[96:97]\n"
    "v_cvt_pk_bf16_f32 v166, v44, v89\n"
    "v_cvt_pk_bf16_f32 v167, v90, v91\n"
    "ds_write_b128 v26, v[164:167] offset:10240\n"
    "v_fmac_f32 v96, v41, v76\n"
    "v_fmac_f32 v97, v41, v77\n"
    "v_fmac_f32 v98, v41, v78\n"
    "v_fmac_f32 v99, v41, v79\n"
    "v_fmac_f32 v100, v41, v80\n"
    "v_fmac_f32 v101, v41, v81\n"
    "v_fmac_f32 v102, v41, v82\n"
    "v_fmac_f32 v103, v41, v83\n"
    "v_fmac_f32 v104, v41, v84\n"
    "v_fmac_f32 v105, v41, v85\n"
    "v_fmac_f32 v106, v41, v86\n"
    "v_fmac_f32 v107, v41, v87\n"
    "v_fmac_f32 v108, v41, v88\n"
    "v_fmac_f32 v109, v41, v89\n"
    "v_fmac_f32 v110, v41, v90\n"
    "v_fmac_f32 v111, v41, v91\n"
    "v_cvt_pk_bf16_f32 v160, -v96, -v97\n"
    "v_cvt_pk_bf16_f32 v161, -v98, -v99\n"
    "v_cvt_pk_bf16_f32 v162, -v100, -v101\n"
    "v_cvt_pk_bf16_f32 v163, -v102, -v103\n"
    "ds_write_b128 v195, v[160:163] offset:0\n"
    "v_cvt_pk_bf16_f32 v164, -v104, -v105\n"
    "v_cvt_pk_bf16_f32 v165, -v106, -v107\n"
    "v_cndmask_b32 v44, v108, 0, s[96:97]\n"
    "v_cvt_pk_bf16_f32 v166, -v44, -v109\n"
    "v_cvt_pk_bf16_f32 v167, -v110, -v111\n"
    "s_waitcnt vmcnt(8)\n"
    "s_mov_b32 s73, 524352\n"
    "buffer_store_dwordx2 v[46:47], v16, s[48:51], s73 offen offset:0 sc1\n"
    "ds_write_b128 v195, v[164:167] offset:2048\n"
    "v_fmac_f32 v76, v40, v96\n"
    "v_fmac_f32 v77, v40, v97\n"
    "v_fmac_f32 v78, v40, v98\n"
    "v_fmac_f32 v79, v40, v99\n"
    "v_fmac_f32 v80, v40, v100\n"
    "v_fmac_f32 v81, v40, v101\n"
    "v_fmac_f32 v82, v40, v102\n"
    "v_fmac_f32 v83, v40, v103\n"
    "v_fmac_f32 v84, v40, v104\n"
    "v_fmac_f32 v85, v40, v105\n"
    "v_fmac_f32 v86, v40, v106\n"
    "v_fmac_f32 v87, v40, v107\n"
    "v_fmac_f32 v88, v40, v108\n"
    "v_fmac_f32 v89, v40, v109\n"
    "v_fmac_f32 v90, v40, v110\n"
    "v_fmac_f32 v91, v40, v111\n"
    "v_cvt_pk_bf16_f32 v160, -v76, -v77\n"
    "v_cvt_pk_bf16_f32 v161, -v78, -v79\n"
    "v_cvt_pk_bf16_f32 v162, -v80, -v81\n"
    "v_cvt_pk_bf16_f32 v163, -v82, -v83\n"
    "ds_write_b128 v206, v[160:163] offset:0\n"
    "v_cvt_pk_bf16_f32 v164, -v84, -v85\n"
    "v_cvt_pk_bf16_f32 v165, -v86, -v87\n"
    "v_cndmask_b32 v44, v88, 0, s[96:97]\n"
    "v_cvt_pk_bf16_f32 v166, -v44, -v89\n"
    "v_cvt_pk_bf16_f32 v167, -v90, -v91\n"
    "s_waitcnt lgkmcnt(13)\n"
    "ds_write_b128 v206, v[164:167] offset:2048\n"
    "v_fmamk_f32 v96, v24, 0x00000000, v25\n"
    "v_fmamk_f32 v97, v24, 0x40800000, v25\n"
    "v_fmamk_f32 v98, v24, 0x41000000, v25\n"
    "v_fmamk_f32 v99, v24, 0x41400000, v25\n"
    "v_fmamk_f32 v100, v24, 0x42000000, v25\n"
    "v_fmamk_f32 v101, v24, 0x42100000, v25\n"
    "v_fmamk_f32 v102, v24, 0x42200000, v25\n"
    "v_fmamk_f32 v103, v24, 0x42300000, v25\n"
    "v_fmamk_f32 v104, v24, 0x42800000, v25\n"
    "v_fmamk_f32 v105, v24, 0x42880000, v25\n"
    "v_fmamk_f32 v106, v24, 0x42900000, v25\n"
    "v_fmamk_f32 v107, v24, 0x42980000, v25\n"
    "v_fmamk_f32 v108, v24, 0x42c00000, v25\n"
    "v_fmamk_f32 v109, v24, 0x42c80000, v25\n"
    "v_fmamk_f32 v110, v24, 0x42d00000, v25\n"
    "v_fmamk_f32 v111, v24, 0x42d80000, v25\n"
    "v_sin_f32 v96, v96\n"
    "v_sin_f32 v97, v97\n"
    "v_sin_f32 v98, v98\n"
    "v_sin_f32 v99, v99\n"
    "v_sin_f32 v100, v100\n"
    "v_sin_f32 v101, v101\n"
    "v_sin_f32 v102, v102\n"
    "v_sin_f32 v103, v103\n"
    "v_sin_f32 v104, v104\n"
    "v_sin_f32 v105, v105\n"
    "v_sin_f32 v106, v106\n"
    "v_sin_f32 v107, v107\n"
    "v_sin_f32 v108, v108\n"
    "v_sin_f32 v109, v109\n"
    "v_sin_f32 v110, v110\n"
    "v_sin_f32 v111, v111\n"
    "v_fmamk_f32 v76, v24, 0x3f800000, v25\n"
    "v_fmamk_f32 v77, v24, 0x40a00000, v25\n"
    "v_fmamk_f32 v78, v24, 0x41100000, v25\n"
    "v_fmamk_f32 v79, v24, 0x41500000, v25\n"
    "v_fmamk_f32 v80, v24, 0x42040000, v25\n"
    "v_fmamk_f32 v81, v24, 0x42140000, v25\n"
    "v_fmamk_f32 v82, v24, 0x42240000, v25\n"
    "v_fmamk_f32 v83, v24, 0x42340000, v25\n"
    "v_fmamk_f32 v84, v24, 0x42820000, v25\n"
    "v_fmamk_f32 v85, v24, 0x428a0000, v25\n"
    "v_fmamk_f32 v86, v24, 0x42920000, v25\n"
    "v_fmamk_f32 v87, v24, 0x429a0000, v25\n"
    "v_fmamk_f32 v88, v24, 0x42c20000, v25\n"
    "v_fmamk_f32 v89, v24, 0x42ca0000, v25\n"
    "v_fmamk_f32 v90, v24, 0x42d20000, v25\n"
    "v_fmamk_f32 v91, v24, 0x42da0000, v25\n"
    "v_sin_f32 v76, v76\n"
    "v_sin_f32 v77, v77\n"
    "v_sin_f32 v78, v78\n"
    "v_sin_f32 v79, v79\n"
    "v_sin_f32 v80, v80\n"
    "v_sin_f32 v81, v81\n"
    "v_sin_f32 v82, v82\n"
    "v_sin_f32 v83, v83\n"
    "v_sin_f32 v84, v84\n"
    "v_sin_f32 v85, v85\n"
    "v_sin_f32 v86, v86\n"
    "v_sin_f32 v87, v87\n"
    "v_sin_f32 v88, v88\n"
    "v_sin_f32 v89, v89\n"
    "v_sin_f32 v90, v90\n"
    "v_sin_f32 v91, v91\n"
    "v_cvt_pk_bf16_f32 v160, -v96, -v97\n"
    "v_cvt_pk_bf16_f32 v161, -v98, -v99\n"
    "v_cvt_pk_bf16_f32 v162, -v100, -v101\n"
    "v_cvt_pk_bf16_f32 v163, -v102, -v103\n"
    "ds_write_b128 v26, v[160:163] offset:4096\n"
    "v_cvt_pk_bf16_f32 v164, -v104, -v105\n"
    "v_cvt_pk_bf16_f32 v165, -v106, -v107\n"
    "v_cndmask_b32 v44, v108, v45, s[96:97]\n"
    "v_cvt_pk_bf16_f32 v166, -v44, -v109\n"
    "v_cvt_pk_bf16_f32 v167, -v110, -v111\n"
    "s_waitcnt vmcnt(8) lgkmcnt(13)\n"
    "v_mfma_f32_32x32x16_bf16 v[176:191], a[240:243], a[16:19], v[176:191]\n"
    "ds_read_b64_tr_b16 a[224:225], v6 offset:16384\n"
    "s_cmp_le_u32 s71, 2\ns_addc_u32 s58, 2, 0\n"
    "s_mul_i32 s58, s58, 8192\n"
    "buffer_load_dwordx4 a[48:51], v11, s[48:51], s58 offen offset:0 sc1\n"
    "v_mfma_f32_32x32x16_bf16 v[208:223], a[244:247], a[16:19], v[208:223]\n"
    "ds_read_b64_tr_b16 a[226:227], v6 offset:17408\n"
    "s_waitcnt lgkmcnt(13)\n"
    "ds_write_b128 v26, v[164:167] offset:6144\n"
    "v_cvt_pk_bf16_f32 v160, -v76, -v77\n"
    "v_cvt_pk_bf16_f32 v161, -v78, -v79\n"
    "v_cvt_pk_bf16_f32 v162, -v80, -v81\n"
    "v_cvt_pk_bf16_f32 v163, -v82, -v83\n"
    "s_waitcnt vmcnt(8)\n"
    "v_mfma_f32_32x32x16_bf16 a[112:127], a[240:243], a[20:23], a[112:127]\n"
    "ds_read_b64_tr_b16 a[228:229], v194 offset:16384\n"
    "buffer_load_dwordx4 a[52:55], v10, s[48:51], s58 offen offset:0 sc1\n"
    "s_waitcnt lgkmcnt(13)\n"
    "ds_write_b128 v26, v[160:163] offset:12288\n"
    "v_mfma_f32_32x32x16_bf16 a[128:143], a[244:247], a[20:23], a[128:143]\n"
    "ds_read_b64_tr_b16 a[230:231], v194 offset:17408\n"
    "v_cvt_pk_bf16_f32 v164, -v84, -v85\n"
    "v_cvt_pk_bf16_f32 v165, -v86, -v87\n"
    "v_cndmask_b32 v44, v88, 0, s[96:97]\n"
    "v_cvt_pk_bf16_f32 v166, -v44, -v89\n"
    "v_cvt_pk_bf16_f32 v167, -v90, -v91\n"
    "s_waitcnt vmcnt(8)\n"
    "v_mfma_f32_32x32x16_bf16 v[176:191], a[248:251], a[24:27], v[176:191]\n"
    "s_waitcnt lgkmcnt(13)\n"
    "ds_read_b64_tr_b16 a[232:233], v6 offset:18432\n"
    "buffer_load_dwordx4 a[56:59], v11, s[48:51], s58 offen offset:1024 sc1\n"
    "ds_write_b128 v26, v[164:167] offset:14336\n"
    "v_mfma_f32_32x32x16_bf16 v[208:223], a[252:255], a[24:27], v[208:223]\n"
    "s_waitcnt lgkmcnt(13)\n"
    "ds_read_b64_tr_b16 a[234:235], v6 offset:19456\n"
    "v_fmac_f32 v96, v41, v76\n"
    "v_fmac_f32 v97, v41, v77\n"
    "v_fmac_f32 v98, v41, v78\n"
    "v_fmac_f32 v99, v41, v79\n"
    "v_fmac_f32 v100, v41, v80\n"
    "v_fmac_f32 v101, v41, v81\n"
    "v_fmac_f32 v102, v41, v82\n"
    "v_fmac_f32 v103, v41, v83\n"
    "v_fmac_f32 v104, v41, v84\n"
    "v_fmac_f32 v105, v41, v85\n"
    "s_waitcnt vmcnt(8)\n"
    "v_mfma_f32_32x32x16_bf16 a[112:127], a[248:251], a[28:31], a[112:127]\n"
    "ds_read_b64_tr_b16 a[236:237], v194 offset:18432\n"
    "buffer_load_dwordx4 a[60:63], v10, s[48:51], s58 offen offset:1024 sc1\n"
    "v_fmac_f32 v106, v41, v86\n"
    "v_fmac_f32 v107, v41, v87\n"
    "v_fmac_f32 v108, v41, v88\n"
    "v_mfma_f32_32x32x16_bf16 a[128:143], a[252:255], a[28:31], a[128:143]\n"
    "s_waitcnt lgkmcnt(13)\n"
    "ds_read_b64_tr_b16 a[238:239], v194 offset:19456\n"
    "v_fmac_f32 v109, v41, v89\n"
    "v_fmac_f32 v110, v41, v90\n"
    "v_fmac_f32 v111, v41, v91\n"
    "v_cvt_pk_bf16_f32 v160, v96, v97\n"
    "v_cvt_pk_bf16_f32 v161, v98, v99\n"
    "v_cvt_pk_bf16_f32 v162, v100, v101\n"
    "v_cvt_pk_bf16_f32 v163, v102, v103\n"
    "s_waitcnt vmcnt(8) lgkmcnt(5)\n"
    "v_mfma_f32_32x32x16_bf16 v[176:191], a[224:227], a[32:35], v[176:191]\n"
    "ds_read_b64_tr_b16 a[240:241], v5 offset:16384\n"
    "s_cmp_le_u32 s71, 3\ns_addc_u32 s58, 3, 0\n"
    "s_mul_i32 s58, s58, 8192\n"
    "buffer_load_dwordx4 a[64:67], v11, s[48:51], s58 offen offset:0 sc1\n"
    "v_mfma_f32_32x32x16_bf16 v[208:223], a[228:231], a[32:35], v[208:223]\n"
    "ds_read_b64_tr_b16 a[242:243], v5 offset:17408\n"
    "ds_write_b128 v195, v[160:163] offset:4096\n"
    "v_cvt_pk_bf16_f32 v164, v104, v105\n"
    "v_cvt_pk_bf16_f32 v165, v106, v107\n"
    "v_cndmask_b32 v44, v108, 0, s[96:97]\n"
    "v_cvt_pk_bf16_f32 v166, v44, v109\n"
    "v_cvt_pk_bf16_f32 v167, v110, v111\n"
    "s_waitcnt vmcnt(8)\n"
    "v_mfma_f32_32x32x16_bf16 a[112:127], a[224:227], a[36:39], a[112:127]\n"
    "ds_read_b64_tr_b16 a[244:245], v8 offset:16384\n"
    "buffer_load_dwordx4 a[68:71], v10, s[48:51], s58 offen offset:0 sc1\n"
    "ds_write_b128 v195, v[164:167] offset:6144\n"
    "v_mfma_f32_32x32x16_bf16 a[128:143], a[228:231], a[36:39], a[128:143]\n"
    "ds_read_b64_tr_b16 a[246:247], v8 offset:17408\n"
    "v_fmac_f32 v76, v40, v96\n"
    "v_fmac_f32 v77, v40, v97\n"
    "v_fmac_f32 v78, v40, v98\n"
    "v_fmac_f32 v79, v40, v99\n"
    "v_fmac_f32 v80, v40, v100\n"
    "v_fmac_f32 v81, v40, v101\n"
    "v_fmac_f32 v82, v40, v102\n"
    "v_fmac_f32 v83, v40, v103\n"
    "v_fmac_f32 v84, v40, v104\n"
    "s_waitcnt vmcnt(8) lgkmcnt(6)\n"
    "v_mfma_f32_32x32x16_bf16 v[176:191], a[232:235], a[40:43], v[176:191]\n"
    "ds_read_b64_tr_b16 a[248:249], v5 offset:18432\n"
    "buffer_load_dwordx2 v[22:23], v14, s[48:51], 0 offen offset:64 sc1\n"
    "v_fmac_f32 v85, v40, v105\n"
    "v_fmac_f32 v86, v40, v106\n"
    "v_fmac_f32 v87, v40, v107\n"
    "v_mfma_f32_32x32x16_bf16 v[208:223], a[236:239], a[40:43], v[208:223]\n"
    "ds_read_b64_tr_b16 a[250:251], v5 offset:19456\n"
    "v_fmac_f32 v88, v40, v108\n"
    "v_fmac_f32 v89, v40, v109\n"
    "v_fmac_f32 v90, v40, v110\n"
    "v_fmac_f32 v91, v40, v111\n"
    "v_cvt_pk_bf16_f32 v160, v76, v77\n"
    "v_cvt_pk_bf16_f32 v161, v78, v79\n"
    "v_cvt_pk_bf16_f32 v162, v80, v81\n"
    "v_cvt_pk_bf16_f32 v163, v82, v83\n"
    "s_waitcnt vmcnt(8)\n"
    "v_mfma_f32_32x32x16_bf16 a[112:127], a[232:235], a[44:47], a[112:127]\n"
    "ds_read_b64_tr_b16 a[252:253], v8 offset:18432\n"
    "buffer_load_dwordx4 a[72:75], v11, s[48:51], s58 offen offset:1024 sc1\n"
    "ds_write_b128 v206, v[160:163] offset:4096\n"
    "v_mfma_f32_32x32x16_bf16 a[128:143], a[236:239], a[44:47], a[128:143]\n"
    "ds_read_b64_tr_b16 a[254:255], v8 offset:19456\n"
    "v_cvt_pk_bf16_f32 v164, v84, v85\n"
    "v_cvt_pk_bf16_f32 v165, v86, v87\n"
    "v_cndmask_b32 v44, v88, 0, s[96:97]\n"
    "v_cvt_pk_bf16_f32 v166, v44, v89\n"
    "v_cvt_pk_bf16_f32 v167, v90, v91\n"
    "ds_write_b128 v206, v[164:167] offset:6144\n"
    "s_mul_i32 s59, s35, 896\n"
    "s_lshl_b32 s60, s71, 7\n"
    "s_add_u32 s59, s59, s60\n"
    "v_lshrrev_b32 v12, 3, v0\n"
    "v_and_b32 v13, 7, v0\n"
    "v_xor_b32 v13, v13, v12\n"
    "v_lshl_add_u32 v12, s34, 4, v12\n"
    "v_mul_u32_u24 v12, 57344, v12\n"
    "v_lshl_add_u32 v12, v13, 4, v12\n"
    "v_add_u32 v13, s59, v12\n"
    "s_lshl_b32 s60, s34, 6\n"
    "s_add_u32 s59, s59, s60\n"
    "v_mul_u32_u24 v12, 57344, v20\n"
    "v_lshl_add_u32 v12, v21, 4, v12\n"
    "v_add_u32 v1, s59, v12\n"
    "v_lshlrev_b32 v12, 2, s34\n"
    "v_and_b32 v193, 7, v20\n"
    "v_xor_b32 v12, v12, v193\n"
    "v_lshlrev_b32 v12, 4, v12\n"
    "v_lshl_add_u32 v12, v21, 3, v12\n"
    "v_lshl_add_u32 v12, v20, 7, v12\n"
    "s_waitcnt vmcnt(7) lgkmcnt(6)\n"
    "v_mfma_f32_32x32x16_bf16 v[176:191], a[240:243], a[48:51], v[176:191]\n"
    "ds_read_b64_tr_b16 a[224:225], v5 offset:32768\n"
    "buffer_load_dwordx4 a[76:79], v10, s[48:51], s58 offen offset:1024 sc1\n"
    "s_lshl_b32 s59, s33, 12\n"
    "s_add_u32 s59, s59, 114688\n"
    "v_add_u32 v193, s59, v12\n"
    "s_lshl_b32 s60, s34, 11\n"
    "s_add_u32 s47, s59, s60\n"
    "s_lshl_b32 s59, s72, 2\n"
    "s_add_u32 s59, s59, s67\n"
    "s_lshl_b32 s59, s59, 12\n"
    "s_add_u32 s59, s59, 14680064\n"
    "v_mfma_f32_32x32x16_bf16 v[208:223], a[244:247], a[48:51], v[208:223]\n"
    "ds_read_b64_tr_b16 a[226:227], v5 offset:33792\n"
    "v_lshl_add_u32 v9, v0, 4, s59\n"
    "s_mul_i32 s59, s70, -14\n"
    "s_add_u32 s59, s59, s72\n"
    "s_add_u32 s59, s59, 7\n"
    "s_lshl_b32 s59, s59, 2\n"
    "s_add_u32 s59, s59, s67\n"
    "s_lshl_b32 s59, s59, 12\n"
    "s_add_u32 s59, s59, 14680064\n"
    "v_lshl_add_u32 v4, v0, 4, s59\n"
    "s_mov_b32 s59, 0x14000\n"
    "s_cmp_eq_u32 s33, 0\ns_cselect_b32 s59, s59, 0x4000\n"
    "s_mov_b32 s60, 0x8000\n"
    "s_cmp_eq_u32 s33, 0\ns_cselect_b32 s60, s60, 0x18000\n"
    "s_cmp_eq_u32 s34, 0\ns_cselect_b32 s59, s59, s60\n"
    "v_lshl_add_u32 v2, v0, 4, s59\n"
    "s_mov_b32 s59, 0x8000\n"
    "s_waitcnt vmcnt(7)\n"
    "v_mfma_f32_32x32x16_bf16 a[112:127], a[240:243], a[52:55], a[112:127]\n"
    "ds_read_b64_tr_b16 a[228:229], v8 offset:32768\n"
    "s_cmp_le_u32 s71, 4\ns_addc_u32 s58, 4, 0\n"
    "s_mul_i32 s58, s58, 8192\n"
    "buffer_load_dwordx4 a[80:83], v11, s[48:51], s58 offen offset:0 sc1\n"
    "s_cmp_eq_u32 s33, 0\ns_cselect_b32 s59, s59, 0x18000\n"
    "s_mov_b32 s60, 0x14000\n"
    "s_cmp_eq_u32 s33, 0\ns_cselect_b32 s60, s60, 0x4000\n"
    "s_cmp_eq_u32 s34, 0\ns_cselect_b32 s59, s59, s60\n"
    "v_mfma_f32_32x32x16_bf16 a[128:143], a[244:247], a[52:55], a[128:143]\n"
    "ds_read_b64_tr_b16 a[230:231], v8 offset:33792\n"
    "v_lshl_add_u32 v3, v0, 4, s59\n"
    "s_waitcnt vmcnt(7) lgkmcnt(5)\n"
    "v_mfma_f32_32x32x16_bf16 v[176:191], a[248:251], a[56:59], v[176:191]\n"
    "ds_read_b64_tr_b16 a[232:233], v5 offset:34816\n"
    "buffer_load_dwordx4 a[84:87], v10, s[48:51], s58 offen offset:0 sc1\n"
    "v_mfma_f32_32x32x16_bf16 v[208:223], a[252:255], a[56:59], v[208:223]\n"
    "ds_read_b64_tr_b16 a[234:235], v5 offset:35840\n"
    "s_waitcnt vmcnt(7)\n"
    "v_mfma_f32_32x32x16_bf16 a[112:127], a[248:251], a[60:63], a[112:127]\n"
    "ds_read_b64_tr_b16 a[236:237], v8 offset:34816\n"
    "buffer_load_dwordx4 a[88:91], v11, s[48:51], s58 offen offset:1024 sc1\n"
    "v_mfma_f32_32x32x16_bf16 a[128:143], a[252:255], a[60:63], a[128:143]\n"
    "ds_read_b64_tr_b16 a[238:239], v8 offset:35840\n"
    "s_waitcnt vmcnt(7) lgkmcnt(4)\n"
    "v_mfma_f32_32x32x16_bf16 v[176:191], a[224:227], a[64:67], v[176:191]\n"
    "ds_read_b64_tr_b16 a[240:241], v6 offset:32768\n"
    "s_waitcnt vmcnt(5)\n"
    "v_cmp_ne_u32 vcc, s62, v22\n"
    "v_cmp_ne_u32 s[64:65], s63, v23\n"
    "s_nop 3\n"
    "s_or_b64 vcc, vcc, s[64:65]\n"
    "s_cbranch_vccnz .Lm_h1_slow\n"
    ".Lm_h1_ok:\n"
    "buffer_load_dwordx4 a[92:95], v10, s[48:51], s58 offen offset:1024 sc1\n"
    "v_mfma_f32_32x32x16_bf16 v[208:223], a[228:231], a[64:67], v[208:223]\n"
    "ds_read_b64_tr_b16 a[242:243], v6 offset:33792\n"
    "s_cmp_le_u32 s71, 5\ns_addc_u32 s58, 5, 0\n"
    "s_mul_i32 s58, s58, 8192\n"
    "buffer_load_dwordx4 a[96:99], v11, s[48:51], s58 offen offset:0 sc1\n"
    "buffer_load_dwordx4 a[100:103], v10, s[48:51], s58 offen offset:0 sc1\n"
    "v_mfma_f32_32x32x16_bf16 a[112:127], a[224:227], a[68:71], a[112:127]\n"
    "ds_read_b64_tr_b16 a[244:245], v194 offset:32768\n"
    "v_mfma_f32_32x32x16_bf16 a[128:143], a[228:231], a[68:71], a[128:143]\n"
    "ds_read_b64_tr_b16 a[246:247], v194 offset:33792\n"
    "buffer_load_dwordx4 a[104:107], v11, s[48:51], s58 offen offset:1024 sc1\n"
    "s_waitcnt vmcnt(8) lgkmcnt(4)\n"
    "v_mfma_f32_32x32x16_bf16 v[176:191], a[232:235], a[72:75], v[176:191]\n"
    "ds_read_b64_tr_b16 a[248:249], v6 offset:34816\n"
    "v_mfma_f32_32x32x16_bf16 v[208:223], a[236:239], a[72:75], v[208:223]\n"
    "ds_read_b64_tr_b16 a[250:251], v6 offset:35840\n"
    "s_waitcnt vmcnt(7)\n"
    "v_mfma_f32_32x32x16_bf16 a[112:127], a[232:235], a[76:79], a[112:127]\n"
    "ds_read_b64_tr_b16 a[252:253], v194 offset:34816\n"
    "buffer_load_dwordx4 a[108:111], v10, s[48:51], s58 offen offset:1024 sc1\n"
    "v_mfma_f32_32x32x16_bf16 a[128:143], a[236:239], a[76:79], a[128:143]\n"
    "ds_read_b64_tr_b16 a[254:255], v194 offset:35840\n"
    "s_waitcnt vmcnt(7) lgkmcnt(4)\n"
    "v_mfma_f32_32x32x16_bf16 v[176:191], a[240:243], a[80:83], v[176:191]\n"
    "ds_read_b64_tr_b16 a[224:225], v6 offset:49152\n"
    "s_cmp_le_u32 s71, 0\ns_addc_u32 s58, 0, 0\n"
    "s_mul_i32 s58, s58, 8192\n"
    "s_or_b32 s58, s58, 4096\n"
    "buffer_load_dwordx4 a[0:3], v11, s[48:51], s58 offen offset:0 sc1\n"
    "v_mfma_f32_32x32x16_bf16 v[208:223], a[244:247], a[80:83], v[208:223]\n"
    "ds_read_b64_tr_b16 a[226:227], v6 offset:50176\n"
    "s_waitcnt vmcnt(7)\n"
    "v_mfma_f32_32x32x16_bf16 a[112:127], a[240:243], a[84:87], a[112:127]\n"
    "ds_read_b64_tr_b16 a[228:229], v194 offset:49152\n"
    "buffer_load_dwordx4 a[4:7], v11, s[48:51], s58 offen offset:1024 sc1\n"
    "v_mfma_f32_32x32x16_bf16 a[128:143], a[244:247], a[84:87], a[128:143]\n"
    "ds_read_b64_tr_b16 a[230:231], v194 offset:50176\n"
    "s_waitcnt vmcnt(7) lgkmcnt(4)\n"
    "v_mfma_f32_32x32x16_bf16 v[176:191], a[248:251], a[88:91], v[176:191]\n"
    "ds_read_b64_tr_b16 a[232:233], v6 offset:51200\n"
    "s_cmp_le_u32 s71, 1\ns_addc_u32 s58, 1, 0\n"
    "s_mul_i32 s58, s58, 8192\n"
    "s_or_b32 s58, s58, 4096\n"
    "buffer_load_dwordx4 a[8:11], v11, s[48:51], s58 offen offset:0 sc1\n"
    "v_mfma_f32_32x32x16_bf16 v[208:223], a[252:255], a[88:91], v[208:223]\n"
    "ds_read_b64_tr_b16 a[234:235], v6 offset:52224\n"
    "s_waitcnt vmcnt(7)\n"
    "v_mfma_f32_32x32x16_bf16 a[112:127], a[248:251], a[92:95], a[112:127]\n"
    "ds_read_b64_tr_b16 a[236:237], v194 offset:51200\n"
    "buffer_load_dwordx4 a[12:15], v11, s[48:51], s58 offen offset:1024 sc1\n"
    "v_mfma_f32_32x32x16_bf16 a[128:143], a[252:255], a[92:95], a[128:143]\n"
    "ds_read_b64_tr_b16 a[238:239], v194 offset:52224\n"
    "s_barrier\n"
    "s_waitcnt vmcnt(7) lgkmcnt(4)\n"
    "v_mfma_f32_32x32x16_bf16 v[176:191], a[224:227], a[96:99], v[176:191]\n"
    "ds_read_b64_tr_b16 a[240:241], v5 offset:8192\n"
    "buffer_load_dwordx2 v[204:205], v15, s[48:51], 0 offen offset:0 sc1\n"
    "ds_read_b128 a[176:179], v27 offset:0\n"
    "ds_read_b128 a[192:195], v27 offset:4096\n"
    "v_mfma_f32_32x32x16_bf16 v[208:223], a[228:231], a[96:99], v[208:223]\n"
    "ds_read_b64_tr_b16 a[242:243], v5 offset:9216\n"
    "s_cmp_le_u32 s71, 2\ns_addc_u32 s58, 2, 0\n"
    "s_mul_i32 s58, s58, 8192\n"
    "s_or_b32 s58, s58, 4096\n"
    "buffer_load_dwordx4 a[16:19], v11, s[48:51], s58 offen offset:0 sc1\n"
    "ds_read_b128 a[180:183], v30 offset:0\n"
    "s_waitcnt vmcnt(8)\n"
    "v_mfma_f32_32x32x16_bf16 a[112:127], a[224:227], a[100:103], a[112:127]\n"
    "ds_read_b64_tr_b16 a[244:245], v8 offset:8192\n"
    "ds_read_b128 a[196:199], v30 offset:4096\n"
    "v_mfma_f32_32x32x16_bf16 a[128:143], a[228:231], a[100:103], a[128:143]\n"
    "ds_read_b64_tr_b16 a[246:247], v8 offset:9216\n"
    "ds_read_b128 a[184:187], v27 offset:2048\n"
    "ds_read_b128 a[200:203], v27 offset:6144\n"
    "s_waitcnt vmcnt(7) lgkmcnt(10)\n"
    "v_mfma_f32_32x32x16_bf16 v[176:191], a[232:235], a[104:107], v[176:191]\n"
    "ds_read_b64_tr_b16 a[248:249], v5 offset:10240\n"
    "buffer_load_dwordx4 a[20:23], v11, s[48:51], s58 offen offset:1024 sc1\n"
    "ds_read_b128 a[188:191], v30 offset:2048\n"
    "ds_read_b128 a[204:207], v30 offset:6144\n"
    "v_mfma_f32_32x32x16_bf16 v[208:223], a[236:239], a[104:107], v[208:223]\n"
    "ds_read_b64_tr_b16 a[250:251], v5 offset:11264\n"
    "s_waitcnt vmcnt(7)\n"
    "v_mfma_f32_32x32x16_bf16 a[112:127], a[232:235], a[108:111], a[112:127]\n"
    "ds_read_b64_tr_b16 a[252:253], v8 offset:10240\n"
    "s_cmp_le_u32 s71, 3\ns_addc_u32 s58, 3, 0\n"
    "s_mul_i32 s58, s58, 8192\n"
    "s_or_b32 s58, s58, 4096\n"
    "buffer_load_dwordx4 a[24:27], v11, s[48:51], s58 offen offset:0 sc1\n"
    "v_mfma_f32_32x32x16_bf16 a[128:143], a[236:239], a[108:111], a[128:143]\n"
    "s_waitcnt lgkmcnt(13)\n"
    "ds_read_b64_tr_b16 a[254:255], v8 offset:11264\n"
    "s_waitcnt vmcnt(7) lgkmcnt(8)\n"
    "v_mfma_f32_32x32x16_bf16 v[176:191], a[240:243], a[0:3], v[176:191]\n"
    "ds_read_b64_tr_b16 a[224:225], v6 offset:24576\n"
    "ds_read_b64_tr_b16 a[226:227], v6 offset:25600\n"
    "buffer_load_dwordx4 a[28:31], v11, s[48:51], s58 offen offset:1024 sc1\n"
    "v_mfma_f32_32x32x16_bf16 v[208:223], a[244:247], a[0:3], v[208:223]\n"
    "ds_read_b64_tr_b16 a[228:229], v194 offset:24576\n"
    "ds_read_b64_tr_b16 a[230:231], v194 offset:25600\n"
    "s_waitcnt vmcnt(7) lgkmcnt(4)\n"
    "v_mfma_f32_32x32x16_bf16 v[176:191], a[248:251], a[4:7], v[176:191]\n"
    "ds_read_b64_tr_b16 a[232:233], v6 offset:26624\n"
    "ds_read_b64_tr_b16 a[234:235], v6 offset:27648\n"
    "s_cmp_le_u32 s71, 4\ns_addc_u32 s58, 4, 0\n"
    "s_mul_i32 s58, s58, 8192\n"
    "s_or_b32 s58, s58, 4096\n"
    "buffer_load_dwordx4 a[32:35], v11, s[48:51], s58 offen offset:0 sc1\n"
    "v_mfma_f32_32x32x16_bf16 v[208:223], a[252:255], a[4:7], v[208:223]\n"
    "ds_read_b64_tr_b16 a[236:237], v194 offset:26624\n"
    "ds_read_b64_tr_b16 a[238:239], v194 offset:27648\n"
    "s_waitcnt vmcnt(7) lgkmcnt(4)\n"
    "v_mfma_f32_32x32x16_bf16 v[176:191], a[224:227], a[8:11], v[176:191]\n"
    "ds_read_b64_tr_b16 a[240:241], v5 offset:24576\n"
    "ds_read_b64_tr_b16 a[242:243], v5 offset:25600\n"
    "s_waitcnt vmcnt(5)\n"
    "v_cmp_ne_u32 vcc, s62, v204\n"
    "v_cmp_ne_u32 s[64:65], s63, v205\n"
    "s_nop 3\n"
    "s_or_b64 vcc, vcc, s[64:65]\n"
    "s_cbranch_vccnz .Lm_u_slow\n"
    ".Lm_u_ok:\n"
    "buffer_load_dwordx4 a[36:39], v11, s[48:51], s58 offen offset:1024 sc1\n"
    "v_mfma_f32_32x32x16_bf16 v[208:223], a[228:231], a[8:11], v[208:223]\n"
    "ds_read_b64_tr_b16 a[244:245], v8 offset:24576\n"
    "ds_read_b64_tr_b16 a[246:247], v8 offset:25600\n"
    "s_cmp_le_u32 s71, 5\ns_addc_u32 s58, 5, 0\n"
    "s_mul_i32 s58, s58, 8192\n"
    "s_or_b32 s58, s58, 4096\n"
    "buffer_load_dwordx4 a[40:43], v11, s[48:51], s58 offen offset:0 sc1\n"
    "s_waitcnt lgkmcnt(4)\n"
    "v_mfma_f32_32x32x16_bf16 v[176:191], a[232:235], a[12:15], v[176:191]\n"
    "ds_read_b64_tr_b16 a[248:249], v5 offset:26624\n"
    "ds_read_b64_tr_b16 a[250:251], v5 offset:27648\n"
    "buffer_load_dwordx4 a[44:47], v11, s[48:51], s58 offen offset:1024 sc1\n"
    "v_mfma_f32_32x32x16_bf16 v[208:223], a[236:239], a[12:15], v[208:223]\n"
    "ds_read_b64_tr_b16 a[252:253], v8 offset:26624\n"
    "ds_read_b64_tr_b16 a[254:255], v8 offset:27648\n"
    "s_cmp_le_u32 s71, 0\ns_addc_u32 s58, 0, 0\n"
    "s_mul_i32 s58, s58, 8192\n"
    "s_or_b32 s58, s58, 4096\n"
    "buffer_load_dwordx4 a[48:51], v10, s[48:51], s58 offen offset:0 sc1\n"
    "s_waitcnt vmcnt(8) lgkmcnt(4)\n"
    "v_mfma_f32_32x32x16_bf16 v[176:191], a[240:243], a[16:19], v[176:191]\n"
    "ds_read_b64_tr_b16 a[224:225], v5 offset:40960\n"
    "ds_read_b64_tr_b16 a[226:227], v5 offset:41984\n"
    "buffer_load_dwordx4 a[52:55], v10, s[48:51], s58 offen offset:1024 sc1\n"
    "v_mfma_f32_32x32x16_bf16 v[208:223], a[244:247], a[16:19], v[208:223]\n"
    "ds_read_b64_tr_b16 a[228:229], v8 offset:40960\n"
    "ds_read_b64_tr_b16 a[230:231], v8 offset:41984\n"
    "s_waitcnt vmcnt(8) lgkmcnt(4)\n"
    "v_mfma_f32_32x32x16_bf16 v[176:191], a[248:251], a[20:23], v[176:191]\n"
    "ds_read_b64_tr_b16 a[232:233], v5 offset:43008\n"
    "ds_read_b64_tr_b16 a[234:235], v5 offset:44032\n"
    "v_mfma_f32_32x32x16_bf16 v[208:223], a[252:255], a[20:23], v[208:223]\n"
    "ds_read_b64_tr_b16 a[236:237], v8 offset:43008\n"
    "ds_read_b64_tr_b16 a[238:239], v8 offset:44032\n"
    "s_mov_b32 s74, 0x0\n"
    "buffer_load_dwordx4 v[64:67], v19, s[48:51], s74 offen offset:0 sc1\n"
    "s_waitcnt vmcnt(8) lgkmcnt(4)\n"
    "v_mfma_f32_32x32x16_bf16 v[176:191], a[224:227], a[24:27], v[176:191]\n"
    "ds_read_b64_tr_b16 a[240:241], v6 offset:40960\n"
    "ds_read_b64_tr_b16 a[242:243], v6 offset:41984\n"
    "v_mfma_f32_32x32x16_bf16 v[208:223], a[228:231], a[24:27], v[208:223]\n"
    "ds_read_b64_tr_b16 a[244:245], v194 offset:40960\n"
    "ds_read_b64_tr_b16 a[246:247], v194 offset:41984\n"
    "buffer_load_dwordx4 v[68:71], v19, s[48:51], s74 offen offset:1024 sc1\n"
    "s_waitcnt vmcnt(8) lgkmcnt(4)\n"
    "v_mfma_f32_32x32x16_bf16 v[176:191], a[232:235], a[28:31], v[176:191]\n"
    "ds_read_b64_tr_b16 a[248:249], v6 offset:43008\n"
    "ds_read_b64_tr_b16 a[250:251], v6 offset:44032\n"
    "v_mfma_f32_32x32x16_bf16 v[208:223], a[236:239], a[28:31], v[208:223]\n"
    "ds_read_b64_tr_b16 a[252:253], v194 offset:43008\n"
    "ds_read_b64_tr_b16 a[254:255], v194 offset:44032\n"
    "buffer_load_dwordx4 v[72:75], v19, s[48:51], s74 offen offset:2048 sc1\n"
    "s_waitcnt vmcnt(8) lgkmcnt(4)\n"
    "v_mfma_f32_32x32x16_bf16 v[176:191], a[240:243], a[32:35], v[176:191]\n"
    "ds_read_b64_tr_b16 a[224:225], v6 offset:57344\n"
    "ds_read_b64_tr_b16 a[226:227], v6 offset:58368\n"
    "v_mfma_f32_32x32x16_bf16 v[208:223], a[244:247], a[32:35], v[208:223]\n"
    "ds_read_b64_tr_b16 a[228:229], v194 offset:57344\n"
    "ds_read_b64_tr_b16 a[230:231], v194 offset:58368\n"
    "s_waitcnt vmcnt(7) lgkmcnt(4)\n"
    "v_mfma_f32_32x32x16_bf16 v[176:191], a[248:251], a[36:39], v[176:191]\n"
    "ds_read_b64_tr_b16 a[232:233], v6 offset:59392\n"
    "ds_read_b64_tr_b16 a[234:235], v6 offset:60416\n"
    "buffer_load_dwordx4 v[76:79], v19, s[48:51], s74 offen offset:3072 sc1\n"
    "v_mfma_f32_32x32x16_bf16 v[208:223], a[252:255], a[36:39], v[208:223]\n"
    "ds_read_b64_tr_b16 a[236:237], v194 offset:59392\n"
    "ds_read_b64_tr_b16 a[238:239], v194 offset:60416\n"
    "s_waitcnt vmcnt(7) lgkmcnt(4)\n"
    "v_mfma_f32_32x32x16_bf16 v[176:191], a[224:227], a[40:43], v[176:191]\n"
    "ds_read_b64_tr_b16 a[240:241], v5 offset:8192\n"
    "ds_read_b64_tr_b16 a[242:243], v5 offset:9216\n"
    "s_mov_b32 s74, 0x1000\n"
    "buffer_load_dwordx4 v[240:243], v19, s[48:51], s74 offen offset:0 sc1\n"
    "v_mfma_f32_32x32x16_bf16 v[208:223], a[228:231], a[40:43], v[208:223]\n"
    "ds_read_b64_tr_b16 a[244:245], v8 offset:8192\n"
    "ds_read_b64_tr_b16 a[246:247], v8 offset:9216\n"
    "s_waitcnt vmcnt(7) lgkmcnt(4)\n"
    "v_mfma_f32_32x32x16_bf16 v[176:191], a[232:235], a[44:47], v[176:191]\n"
    "ds_read_b64_tr_b16 a[248:249], v5 offset:10240\n"
    "ds_read_b64_tr_b16 a[250:251], v5 offset:11264\n"
    "buffer_load_dwordx4 v[244:247], v19, s[48:51], s74 offen offset:1024 sc1\n"
    "v_mfma_f32_32x32x16_bf16 v[208:223], a[236:239], a[44:47], v[208:223]\n"
    "ds_read_b64_tr_b16 a[252:253], v8 offset:10240\n"
    "ds_read_b64_tr_b16 a[254:255], v8 offset:11264\n"
    "s_waitcnt vmcnt(7) lgkmcnt(4)\n"
    "v_mfma_f32_32x32x16_bf16 a[112:127], a[240:243], a[48:51], a[112:127]\n"
    "ds_read_b64_tr_b16 a[224:225], v6 offset:24576\n"
    "ds_read_b64_tr_b16 a[226:227], v6 offset:25600\n"
    "buffer_load_dwordx4 v[248:251], v19, s[48:51], s74 offen offset:2048 sc1\n"
    "v_mfma_f32_32x32x16_bf16 a[128:143], a[244:247], a[48:51], a[128:143]\n"
    "ds_read_b64_tr_b16 a[228:229], v194 offset:24576\n"
    "ds_read_b64_tr_b16 a[230:231], v194 offset:25600\n"
    "s_nop 11\n"
    "ds_write_b128 v2, v[208:211] offset:0\n"
    "s_waitcnt vmcnt(7) lgkmcnt(5)\n"
    "v_mfma_f32_32x32x16_bf16 a[112:127], a[248:251], a[52:55], a[112:127]\n"
    "ds_read_b64_tr_b16 a[232:233], v6 offset:26624\n"
    "ds_read_b64_tr_b16 a[234:235], v6 offset:27648\n"
    "buffer_load_dwordx4 v[252:255], v19, s[48:51], s74 offen offset:3072 sc1\n"
    "s_mov_b32 s74, 0x4000\n"
    "buffer_load_dwordx4 v[128:131], v19, s[48:51], s74 offen offset:0 sc1\n"
    "buffer_load_dwordx4 v[132:135], v19, s[48:51], s74 offen offset:1024 sc1\n"
    "buffer_load_dwordx4 v[136:139], v19, s[48:51], s74 offen offset:2048 sc1\n"
    "buffer_load_dwordx4 v[140:143], v19, s[48:51], s74 offen offset:3072 sc1\n"
    "s_mov_b32 s74, 0x5000\n"
    "buffer_load_dwordx4 v[144:147], v19, s[48:51], s74 offen offset:0 sc1\n"
    "buffer_load_dwordx4 v[148:151], v19, s[48:51], s74 offen offset:1024 sc1\n"
    "buffer_load_dwordx4 v[152:155], v19, s[48:51], s74 offen offset:2048 sc1\n"
    "buffer_load_dwordx4 v[156:159], v19, s[48:51], s74 offen offset:3072 sc1\n"
    "ds_write_b128 v2, v[212:215] offset:1024\n"
    "ds_write_b128 v2, v[216:219] offset:2048\n"
    "s_cmp_le_u32 s71, 1\ns_addc_u32 s58, 1, 0\n"
    "s_mul_i32 s58, s58, 8192\n"
    "s_or_b32 s58, s58, 4096\n"
    "buffer_load_dwordx4 a[56:59], v10, s[48:51], s58 offen offset:0 sc1\n"
    "ds_write_b128 v2, v[220:223] offset:3072\n"
    "v_mfma_f32_32x32x16_bf16 a[128:143], a[252:255], a[52:55], a[128:143]\n"
    "ds_read_b64_tr_b16 a[236:237], v194 offset:26624\n"
    "ds_read_b64_tr_b16 a[238:239], v194 offset:27648\n"
    "s_waitcnt vmcnt(9)\n"
    "v_perm_b32 v80, v68, v64, s75\n"
    "v_perm_b32 v81, v76, v72, s75\n"
    "v_perm_b32 v82, v244, v240, s75\n"
    "buffer_load_dwordx4 a[60:63], v10, s[48:51], s58 offen offset:1024 sc1\n"
    "v_perm_b32 v83, v252, v248, s75\n"
    "v_perm_b32 v84, v68, v64, s40\n"
    "v_perm_b32 v85, v76, v72, s40\n"
    "v_perm_b32 v86, v244, v240, s40\n"
    "v_perm_b32 v87, v252, v248, s40\n"
    "s_cmp_le_u32 s71, 2\ns_addc_u32 s58, 2, 0\n"
    "s_mul_i32 s58, s58, 8192\n"
    "s_or_b32 s58, s58, 4096\n"
    "buffer_load_dwordx4 a[64:67], v10, s[48:51], s58 offen offset:0 sc1\n"
    "buffer_load_dwordx4 a[68:71], v10, s[48:51], s58 offen offset:1024 sc1\n"
    "s_cmp_le_u32 s71, 3\ns_addc_u32 s58, 3, 0\n"
    "s_mul_i32 s58, s58, 8192\n"
    "s_or_b32 s58, s58, 4096\n"
    "buffer_load_dwordx4 a[72:75], v10, s[48:51], s58 offen offset:0 sc1\n"
    "buffer_load_dwordx4 a[76:79], v10, s[48:51], s58 offen offset:1024 sc1\n"
    "s_cmp_le_u32 s71, 4\ns_addc_u32 s58, 4, 0\n"
    "s_mul_i32 s58, s58, 8192\n"
    "s_or_b32 s58, s58, 4096\n"
    "buffer_load_dwordx4 a[80:83], v10, s[48:51], s58 offen offset:0 sc1\n"
    "s_waitcnt vmcnt(7)\n"
    "buffer_load_dwordx4 a[84:87], v10, s[48:51], s58 offen offset:1024 sc1\n"
    "v_perm_b32 v88, v132, v128, s75\n"
    "v_perm_b32 v89, v140, v136, s75\n"
    "v_perm_b32 v90, v148, v144, s75\n"
    "v_perm_b32 v91, v156, v152, s75\n"
    "v_perm_b32 v92, v132, v128, s40\n"
    "v_perm_b32 v93, v140, v136, s40\n"
    "v_perm_b32 v94, v148, v144, s40\n"
    "v_perm_b32 v95, v156, v152, s40\n"
    "s_waitcnt lgkmcnt(2)\n"
    "s_barrier\n"
    "s_waitcnt vmcnt(7)\n"
    "v_mfma_f32_32x32x16_bf16 a[112:127], a[224:227], a[56:59], a[112:127]\n"
    "ds_read_b64_tr_b16 a[240:241], v5 offset:24576\n"
    "ds_read_b64_tr_b16 a[242:243], v5 offset:25600\n"
    "s_cmp_le_u32 s71, 5\ns_addc_u32 s58, 5, 0\n"
    "s_mul_i32 s58, s58, 8192\n"
    "s_or_b32 s58, s58, 4096\n"
    "buffer_load_dwordx4 a[88:91], v10, s[48:51], s58 offen offset:0 sc1\n"
    "v_mfma_f32_32x32x16_bf16 a[128:143], a[228:231], a[56:59], a[128:143]\n"
    "ds_read_b64_tr_b16 a[244:245], v8 offset:24576\n"
    "ds_read_b64_tr_b16 a[246:247], v8 offset:25600\n"
    "buffer_load_dwordx4 a[92:95], v10, s[48:51], s58 offen offset:1024 sc1\n"
    "ds_read_b128 v[160:163], v3 offset:0\n"
    "s_waitcnt vmcnt(8) lgkmcnt(5)\n"
    "v_mfma_f32_32x32x16_bf16 a[112:127], a[232:235], a[60:63], a[112:127]\n"
    "ds_read_b64_tr_b16 a[248:249], v5 offset:26624\n"
    "ds_read_b64_tr_b16 a[250:251], v5 offset:27648\n"
    "ds_read_b128 v[164:167], v3 offset:1024\n"
    "ds_read_b128 v[168:171], v3 offset:2048\n"
    "v_mfma_f32_32x32x16_bf16 a[128:143], a[236:239], a[60:63], a[128:143]\n"
    "ds_read_b64_tr_b16 a[252:253], v8 offset:26624\n"
    "ds_read_b64_tr_b16 a[254:255], v8 offset:27648\n"
    "ds_read_b128 v[172:175], v3 offset:3072\n"
    "s_mov_b32 m0, s47\n"
    "s_mov_b32 s73, 0\n"
    "s_nop 0\n"
    "buffer_load_dwordx4 v13, s[36:39], s73 offen offset:0 lds\n"
    "s_waitcnt vmcnt(8) lgkmcnt(8)\n"
    "v_mfma_f32_32x32x16_bf16 a[112:127], a[240:243], a[64:67], a[112:127]\n"
    "ds_read_b64_tr_b16 a[224:225], v5 offset:40960\n"
    "ds_read_b64_tr_b16 a[226:227], v5 offset:41984\n"
    "v_mfma_f32_32x32x16_bf16 a[128:143], a[244:247], a[64:67], a[128:143]\n"
    "ds_read_b64_tr_b16 a[228:229], v8 offset:40960\n"
    "ds_read_b64_tr_b16 a[230:231], v8 offset:41984\n"
    "v_mfma_f32_32x32x16_bf16 v[96:111], v[80:83], v[32:35], 0\n"
    "s_waitcnt lgkmcnt(4)\n"
    "v_add_f32 v176, v176, v160\n"
    "v_add_f32 v177, v177, v161\n"
    "v_mfma_f32_32x32x16_bf16 v[96:111], v[84:87], v[56:59], v[96:111]\n"
    "v_add_f32 v178, v178, v162\n"
    "v_add_f32 v179, v179, v163\n"
    "v_add_f32 v180, v180, v164\n"
    "v_add_f32 v181, v181, v165\n"
    "v_add_f32 v182, v182, v166\n"
    "v_add_f32 v183, v183, v167\n"
    "v_mfma_f32_32x32x16_bf16 v[96:111], v[88:91], v[36:39], v[96:111]\n"
    "s_mov_b32 s73, 457728\n"
    "buffer_load_dwordx4 v13, s[36:39], s73 offen offset:1024 lds\n"
    "v_add_f32 v184, v184, v168\n"
    "v_add_f32 v185, v185, v169\n"
    "v_add_f32 v186, v186, v170\n"
    "v_add_f32 v187, v187, v171\n"
    "v_mfma_f32_32x32x16_bf16 v[96:111], v[92:95], v[60:63], v[96:111]\n"
    "v_add_f32 v188, v188, v172\n"
    "v_add_f32 v189, v189, v173\n"
    "v_add_f32 v190, v190, v174\n"
    "v_add_f32 v191, v191, v175\n"
    "v_cvt_pk_bf16_f32 v176, v176, v177\n"
    "s_waitcnt vmcnt(8)\n"
    "v_mfma_f32_32x32x16_bf16 a[112:127], a[248:251], a[68:71], a[112:127]\n"
    "ds_read_b64_tr_b16 a[232:233], v5 offset:43008\n"
    "ds_read_b64_tr_b16 a[234:235], v5 offset:44032\n"
    "v_cvt_pk_bf16_f32 v177, v178, v179\n"
    "v_cvt_pk_bf16_f32 v178, v180, v181\n"
    "v_cvt_pk_bf16_f32 v180, v182, v183\n"
    "v_mfma_f32_32x32x16_bf16 a[128:143], a[252:255], a[68:71], a[128:143]\n"
    "ds_read_b64_tr_b16 a[236:237], v8 offset:43008\n"
    "ds_read_b64_tr_b16 a[238:239], v8 offset:44032\n"
    "v_mov_b32 v179, s62\n"
    "v_cvt_pk_bf16_f32 v181, v184, v185\n"
    "v_cvt_pk_bf16_f32 v182, v186, v187\n"
    "v_mfma_f32_32x32x16_bf16 v[112:127], v[80:83], v[48:51], 0\n"
    "v_mov_b32 v183, s62\n"
    "v_cvt_pk_bf16_f32 v184, v188, v189\n"
    "v_cvt_pk_bf16_f32 v185, v190, v191\n"
    "v_mov_b32 v187, s62\n"
    "v_cvt_pk_bf16_f32 v224, v96, v97\n"
    "v_mfma_f32_32x32x16_bf16 v[112:127], v[84:87], v[32:35], v[112:127]\n"
    "buffer_store_dwordx4 v[176:179], v9, s[48:51], 0 offen offset:0 sc1\n"
    "v_mfma_f32_32x32x16_bf16 v[112:127], v[88:91], v[52:55], v[112:127]\n"
    "buffer_store_dwordx4 v[180:183], v9, s[48:51], 0 offen offset:1024 sc1\n"
    "v_mfma_f32_32x32x16_bf16 v[112:127], v[92:95], v[36:39], v[112:127]\n"
    "buffer_store_dwordx4 v[184:187], v9, s[48:51], 0 offen offset:2048 sc1\n"
    "s_waitcnt vmcnt(10) lgkmcnt(4)\n"
    "v_mfma_f32_32x32x16_bf16 a[112:127], a[224:227], a[72:75], a[112:127]\n"
    "ds_read_b64_tr_b16 a[240:241], v6 offset:40960\n"
    "ds_read_b64_tr_b16 a[242:243], v6 offset:41984\n"
    "v_cvt_pk_bf16_f32 v225, v98, v99\n"
    "v_cvt_pk_bf16_f32 v226, v100, v101\n"
    "v_cvt_pk_bf16_f32 v227, v102, v103\n"
    "v_mfma_f32_32x32x16_bf16 a[128:143], a[228:231], a[72:75], a[128:143]\n"
    "ds_read_b64_tr_b16 a[244:245], v194 offset:40960\n"
    "ds_read_b64_tr_b16 a[246:247], v194 offset:41984\n"
    "v_cvt_pk_bf16_f32 v228, v104, v105\n"
    "v_cvt_pk_bf16_f32 v229, v106, v107\n"
    "v_cvt_pk_bf16_f32 v230, v108, v109\n"
    "s_waitcnt vmcnt(9) lgkmcnt(4)\n"
    "v_mfma_f32_32x32x16_bf16 a[112:127], a[232:235], a[76:79], a[112:127]\n"
    "ds_read_b64_tr_b16 a[248:249], v6 offset:43008\n"
    "ds_read_b64_tr_b16 a[250:251], v6 offset:44032\n"
    "v_cvt_pk_bf16_f32 v231, v110, v111\n"
    "s_nop 3\n"
    "v_cvt_pk_bf16_f32 v232, v112, v113\n"
    "v_cvt_pk_bf16_f32 v233, v114, v115\n"
    "v_mfma_f32_32x32x16_bf16 a[128:143], a[236:239], a[76:79], a[128:143]\n"
    "ds_read_b64_tr_b16 a[252:253], v194 offset:43008\n"
    "ds_read_b64_tr_b16 a[254:255], v194 offset:44032\n"
    "v_cvt_pk_bf16_f32 v234, v116, v117\n"
    "v_cvt_pk_bf16_f32 v235, v118, v119\n"
    "v_cvt_pk_bf16_f32 v236, v120, v121\n"
    "v_mfma_f32_32x32x16_bf16 a[144:159], a[176:179], v[224:227], 0\n"
    "v_cvt_pk_bf16_f32 v237, v122, v123\n"
    "v_cvt_pk_bf16_f32 v238, v124, v125\n"
    "v_cvt_pk_bf16_f32 v239, v126, v127\n"
    "v_perm_b32 v80, v69, v65, s75\n"
    "v_mfma_f32_32x32x16_bf16 a[160:175], a[180:183], v[224:227], 0\n"
    "v_perm_b32 v81, v77, v73, s75\n"
    "v_perm_b32 v82, v245, v241, s75\n"
    "v_perm_b32 v83, v253, v249, s75\n"
    "v_perm_b32 v84, v69, v65, s40\n"
    "v_mfma_f32_32x32x16_bf16 a[144:159], a[184:187], v[228:231], a[144:159]\n"
    "v_perm_b32 v85, v77, v73, s40\n"
    "v_perm_b32 v86, v245, v241, s40\n"
    "v_perm_b32 v87, v253, v249, s40\n"
    "v_perm_b32 v88, v133, v129, s75\n"
    "v_mfma_f32_32x32x16_bf16 a[160:175], a[188:191], v[228:231], a[160:175]\n"
    "v_perm_b32 v89, v141, v137, s75\n"
    "v_perm_b32 v90, v149, v145, s75\n"
    "v_perm_b32 v91, v157, v153, s75\n"
    "v_perm_b32 v92, v133, v129, s40\n"
    "v_perm_b32 v93, v141, v137, s40\n"
    "v_perm_b32 v94, v149, v145, s40\n"
    "v_perm_b32 v95, v157, v153, s40\n"
    "v_mfma_f32_32x32x16_bf16 v[96:111], v[80:83], v[32:35], 0\n"
    "ds_read_b128 a[176:179], v27 offset:8192\n"
    "v_mfma_f32_32x32x16_bf16 v[96:111], v[84:87], v[56:59], v[96:111]\n"
    "v_mfma_f32_32x32x16_bf16 v[96:111], v[88:91], v[36:39], v[96:111]\n"
    "ds_read_b128 a[180:183], v30 offset:8192\n"
    "v_mfma_f32_32x32x16_bf16 v[96:111], v[92:95], v[60:63], v[96:111]\n"
    "s_waitcnt vmcnt(8) lgkmcnt(6)\n"
    "v_mfma_f32_32x32x16_bf16 a[112:127], a[240:243], a[80:83], a[112:127]\n"
    "ds_read_b64_tr_b16 a[224:225], v6 offset:57344\n"
    "ds_read_b64_tr_b16 a[226:227], v6 offset:58368\n"
    "ds_read_b128 a[184:187], v27 offset:10240\n"
    "v_mfma_f32_32x32x16_bf16 a[128:143], a[244:247], a[80:83], a[128:143]\n"
    "ds_read_b64_tr_b16 a[228:229], v194 offset:57344\n"
    "ds_read_b64_tr_b16 a[230:231], v194 offset:58368\n"
    "s_nop 11\n"
    "v_cvt_pk_bf16_f32 v224, v96, v97\n"
    "v_cvt_pk_bf16_f32 v225, v98, v99\n"
    "v_cvt_pk_bf16_f32 v226, v100, v101\n"
    "v_mfma_f32_32x32x16_bf16 a[144:159], a[192:195], v[232:235], a[144:159]\n"
    "v_cvt_pk_bf16_f32 v227, v102, v103\n"
    "v_cvt_pk_bf16_f32 v228, v104, v105\n"
    "v_cvt_pk_bf16_f32 v229, v106, v107\n"
    "v_cvt_pk_bf16_f32 v230, v108, v109\n"
    "v_mfma_f32_32x32x16_bf16 a[160:175], a[196:199], v[232:235], a[160:175]\n"
    "v_cvt_pk_bf16_f32 v231, v110, v111\n"
    "ds_read_b128 a[188:191], v30 offset:10240\n"
    "v_mfma_f32_32x32x16_bf16 a[144:159], a[200:203], v[236:239], a[144:159]\n"
    "v_mfma_f32_32x32x16_bf16 a[160:175], a[204:207], v[236:239], a[160:175]\n"
    "ds_read_b128 a[192:195], v27 offset:12288\n"
    "s_waitcnt vmcnt(7) lgkmcnt(9)\n"
    "v_mfma_f32_32x32x16_bf16 a[112:127], a[248:251], a[84:87], a[112:127]\n"
    "ds_read_b64_tr_b16 a[232:233], v6 offset:59392\n"
    "ds_read_b64_tr_b16 a[234:235], v6 offset:60416\n"
    "ds_read_b128 a[196:199], v30 offset:12288\n"
    "v_mfma_f32_32x32x16_bf16 a[128:143], a[252:255], a[84:87], a[128:143]\n"
    "ds_read_b64_tr_b16 a[236:237], v194 offset:59392\n"
    "ds_read_b64_tr_b16 a[238:239], v194 offset:60416\n"
    "v_mfma_f32_32x32x16_bf16 v[112:127], v[80:83], v[48:51], 0\n"
    "ds_read_b128 a[200:203], v27 offset:14336\n"
    "v_mfma_f32_32x32x16_bf16 v[112:127], v[84:87], v[32:35], v[112:127]\n"
    "v_mfma_f32_32x32x16_bf16 v[112:127], v[88:91], v[52:55], v[112:127]\n"
    "s_waitcnt lgkmcnt(13)\n"
    "ds_read_b128 a[204:207], v30 offset:14336\n"
    "v_mfma_f32_32x32x16_bf16 v[112:127], v[92:95], v[36:39], v[112:127]\n"
    "v_perm_b32 v80, v70, v66, s75\n"
    "v_perm_b32 v81, v78, v74, s75\n"
    "v_perm_b32 v82, v246, v242, s75\n"
    "v_perm_b32 v83, v254, v250, s75\n"
    "s_waitcnt vmcnt(6) lgkmcnt(9)\n"
    "v_mfma_f32_32x32x16_bf16 a[112:127], a[224:227], a[88:91], a[112:127]\n"
    "v_perm_b32 v84, v70, v66, s40\n"
    "v_perm_b32 v85, v78, v74, s40\n"
    "v_perm_b32 v86, v246, v242, s40\n"
    "v_perm_b32 v87, v254, v250, s40\n"
    "v_mfma_f32_32x32x16_bf16 a[128:143], a[228:231], a[88:91], a[128:143]\n"
    "s_nop 3\n"
    "v_cvt_pk_bf16_f32 v232, v112, v113\n"
    "v_cvt_pk_bf16_f32 v233, v114, v115\n"
    "v_cvt_pk_bf16_f32 v234, v116, v117\n"
    "v_cvt_pk_bf16_f32 v235, v118, v119\n"
    "s_waitcnt lgkmcnt(8)\n"
    "v_mfma_f32_32x32x16_bf16 a[144:159], a[176:179], v[224:227], a[144:159]\n"
    "buffer_load_dwordx4 v[160:163], v4, s[48:51], 0 offen offset:0 sc1\n"
    "v_cvt_pk_bf16_f32 v236, v120, v121\n"
    "v_cvt_pk_bf16_f32 v237, v122, v123\n"
    "v_cvt_pk_bf16_f32 v238, v124, v125\n"
    "v_mfma_f32_32x32x16_bf16 a[160:175], a[180:183], v[224:227], a[160:175]\n"
    "buffer_load_dwordx4 v[164:167], v4, s[48:51], 0 offen offset:1024 sc1\n"
    "v_cvt_pk_bf16_f32 v239, v126, v127\n"
    "v_perm_b32 v88, v134, v130, s75\n"
    "v_perm_b32 v89, v142, v138, s75\n"
    "v_mfma_f32_32x32x16_bf16 a[144:159], a[184:187], v[228:231], a[144:159]\n"
    "buffer_load_dwordx4 v[168:171], v4, s[48:51], 0 offen offset:2048 sc1\n"
    "v_perm_b32 v90, v150, v146, s75\n"
    "v_perm_b32 v91, v158, v154, s75\n"
    "v_perm_b32 v92, v134, v130, s40\n"
    "v_mfma_f32_32x32x16_bf16 a[160:175], a[188:191], v[228:231], a[160:175]\n"
    "v_perm_b32 v93, v142, v138, s40\n"
    "v_perm_b32 v94, v150, v146, s40\n"
    "v_perm_b32 v95, v158, v154, s40\n"
    "ds_read_b128 a[176:179], v28 offset:0\n"
    "v_mfma_f32_32x32x16_bf16 v[96:111], v[80:83], v[32:35], 0\n"
    "v_mfma_f32_32x32x16_bf16 v[96:111], v[84:87], v[56:59], v[96:111]\n"
    "ds_read_b128 a[180:183], v31 offset:0\n"
    "v_mfma_f32_32x32x16_bf16 v[96:111], v[88:91], v[36:39], v[96:111]\n"
    "v_mfma_f32_32x32x16_bf16 v[96:111], v[92:95], v[60:63], v[96:111]\n"
    "ds_read_b128 a[184:187], v28 offset:2048\n"
    "s_waitcnt vmcnt(8) lgkmcnt(5)\n"
    "v_mfma_f32_32x32x16_bf16 a[112:127], a[232:235], a[92:95], a[112:127]\n"
    "v_mfma_f32_32x32x16_bf16 a[128:143], a[236:239], a[92:95], a[128:143]\n"
    "s_nop 11\n"
    "v_cvt_pk_bf16_f32 v224, v96, v97\n"
    "v_cvt_pk_bf16_f32 v225, v98, v99\n"
    "v_cvt_pk_bf16_f32 v226, v100, v101\n"
    "v_cvt_pk_bf16_f32 v227, v102, v103\n"
    "s_waitcnt lgkmcnt(3)\n"
    "v_mfma_f32_32x32x16_bf16 a[144:159], a[192:195], v[232:235], a[144:159]\n"
    "v_cvt_pk_bf16_f32 v228, v104, v105\n"
    "v_cvt_pk_bf16_f32 v229, v106, v107\n"
    "v_cvt_pk_bf16_f32 v230, v108, v109\n"
    "v_cvt_pk_bf16_f32 v231, v110, v111\n"
    "v_mfma_f32_32x32x16_bf16 a[160:175], a[196:199], v[232:235], a[160:175]\n"
    "ds_read_b128 a[188:191], v31 offset:2048\n"
    "ds_write_b128 v2, a[128:131] offset:4096\n"
    "v_mfma_f32_32x32x16_bf16 a[144:159], a[200:203], v[236:239], a[144:159]\n"
    "v_accvgpr_read_b32 v176, a112\n"
    "v_accvgpr_read_b32 v177, a113\n"
    "v_accvgpr_read_b32 v178, a114\n"
    "v_accvgpr_read_b32 v179, a115\n"
    "v_mfma_f32_32x32x16_bf16 a[160:175], a[204:207], v[236:239], a[160:175]\n"
    "ds_read_b128 a[192:195], v28 offset:4096\n"
    "ds_write_b128 v2, a[132:135] offset:5120\n"
    "v_mfma_f32_32x32x16_bf16 v[112:127], v[80:83], v[48:51], 0\n"
    "v_accvgpr_read_b32 v180, a116\n"
    "ds_read_b128 a[196:199], v31 offset:4096\n"
    "v_accvgpr_read_b32 v181, a117\n"
    "v_accvgpr_read_b32 v182, a118\n"
    "v_mfma_f32_32x32x16_bf16 v[112:127], v[84:87], v[32:35], v[112:127]\n"
    "v_accvgpr_read_b32 v183, a119\n"
    "ds_write_b128 v2, a[136:139] offset:6144\n"
    "v_accvgpr_read_b32 v184, a120\n"
    "v_mfma_f32_32x32x16_bf16 v[112:127], v[88:91], v[52:55], v[112:127]\n"
    "ds_read_b128 a[200:203], v28 offset:6144\n"
    "v_accvgpr_read_b32 v185, a121\n"
    "v_accvgpr_read_b32 v186, a122\n"
    "v_accvgpr_read_b32 v187, a123\n"
    "v_mfma_f32_32x32x16_bf16 v[112:127], v[92:95], v[36:39], v[112:127]\n"
    "ds_write_b128 v2, a[140:143] offset:7168\n"
    "v_accvgpr_read_b32 v188, a124\n"
    "v_accvgpr_read_b32 v189, a125\n"
    "v_accvgpr_read_b32 v190, a126\n"
    "v_accvgpr_read_b32 v191, a127\n"
    "ds_read_b128 a[204:207], v31 offset:6144\n"
    "v_perm_b32 v80, v71, v67, s75\n"
    "v_perm_b32 v81, v79, v75, s75\n"
    "v_perm_b32 v82, v247, v243, s75\n"
    "v_perm_b32 v83, v255, v251, s75\n"
    "v_perm_b32 v84, v71, v67, s40\n"
    "v_perm_b32 v85, v79, v75, s40\n"
    "v_perm_b32 v86, v247, v243, s40\n"
    "v_perm_b32 v87, v255, v251, s40\n"
    "v_perm_b32 v88, v135, v131, s75\n"
    "v_perm_b32 v89, v143, v139, s75\n"
    "v_perm_b32 v90, v151, v147, s75\n"
    "v_perm_b32 v91, v159, v155, s75\n"
    "v_perm_b32 v92, v135, v131, s40\n"
    "v_perm_b32 v93, v143, v139, s40\n"
    "v_perm_b32 v94, v151, v147, s40\n"
    "v_perm_b32 v95, v159, v155, s40\n"
    "s_waitcnt vmcnt(6) lgkmcnt(1)\n"
    "s_barrier\n"
    "v_mfma_f32_32x32x16_bf16 a[144:159], a[176:179], v[224:227], a[144:159]\n"
    "ds_read_b128 v[208:211], v3 offset:4096\n"
    "ds_read_b128 v[212:215], v3 offset:5120\n"
    "ds_read_b128 v[216:219], v3 offset:6144\n"
    "ds_read_b128 v[220:223], v3 offset:7168\n"
    "v_mfma_f32_32x32x16_bf16 a[160:175], a[180:183], v[224:227], a[160:175]\n"
    "v_cvt_pk_bf16_f32 v232, v112, v113\n"
    "v_cvt_pk_bf16_f32 v233, v114, v115\n"
    "v_cvt_pk_bf16_f32 v234, v116, v117\n"
    "v_cvt_pk_bf16_f32 v235, v118, v119\n"
    "ds_read_b64 v[196:197], v193 offset:0\n"
    "v_mfma_f32_32x32x16_bf16 a[144:159], a[184:187], v[228:231], a[144:159]\n"
    "v_cvt_pk_bf16_f32 v236, v120, v121\n"
    "v_cvt_pk_bf16_f32 v237, v122, v123\n"
    "v_cvt_pk_bf16_f32 v238, v124, v125\n"
    "v_cvt_pk_bf16_f32 v239, v126, v127\n"
    "v_xor_b32 v193, 16, v193\n"
    "v_mfma_f32_32x32x16_bf16 a[160:175], a[188:191], v[228:231], a[160:175]\n"
    "ds_read_b128 a[176:179], v29 offset:0\n"
    "ds_read_b64 v[198:199], v193 offset:0\n"
    "v_xor_b32 v193, 48, v193\n"
    "ds_read_b64 v[200:201], v193 offset:0\n"
    "v_xor_b32 v193, 16, v193\n"
    "v_mfma_f32_32x32x16_bf16 v[96:111], v[80:83], v[32:35], 0\n"
    "ds_read_b64 v[202:203], v193 offset:0\n"
    "ds_read_b128 a[180:183], v192 offset:0\n"
    "v_mfma_f32_32x32x16_bf16 v[96:111], v[84:87], v[56:59], v[96:111]\n"
    "v_mfma_f32_32x32x16_bf16 v[96:111], v[88:91], v[36:39], v[96:111]\n"
    "ds_read_b128 a[184:187], v29 offset:2048\n"
    "s_waitcnt vmcnt(0)\n"
    "v_cmp_ne_u32 vcc, s62, v163\n"
    "v_cmp_ne_u32 s[64:65], s62, v167\n"
    "s_nop 3\n"
    "s_or_b64 vcc, vcc, s[64:65]\n"
    "v_cmp_ne_u32 s[64:65], s62, v171\n"
    "s_nop 3\n"
    "s_or_b64 vcc, vcc, s[64:65]\n"
    "v_mfma_f32_32x32x16_bf16 v[96:111], v[92:95], v[60:63], v[96:111]\n"
    "s_cbranch_vccnz .Lm_x_slow\n"
    ".Lm_x_ok:\n"
    "ds_read_b128 a[188:191], v192 offset:2048\n"
    "v_lshlrev_b32 v172, 16, v160\n"
    "v_and_b32 v173, s46, v160\n"
    "v_add_f32 v176, v176, v172\n"
    "s_waitcnt lgkmcnt(12)\n"
    "v_mfma_f32_32x32x16_bf16 a[144:159], a[192:195], v[232:235], a[144:159]\n"
    "v_add_f32 v177, v177, v173\n"
    "v_lshlrev_b32 v172, 16, v161\n"
    "v_and_b32 v173, s46, v161\n"
    "v_add_f32 v178, v178, v172\n"
    "v_add_f32 v179, v179, v173\n"
    "v_lshlrev_b32 v172, 16, v162\n"
    "v_mfma_f32_32x32x16_bf16 a[160:175], a[196:199], v[232:235], a[160:175]\n"
    "s_nop 2\n"
    "v_cvt_pk_bf16_f32 v224, v96, v97\n"
    "v_cvt_pk_bf16_f32 v225, v98, v99\n"
    "v_cvt_pk_bf16_f32 v226, v100, v101\n"
    "v_cvt_pk_bf16_f32 v227, v102, v103\n"
    "v_and_b32 v173, s46, v162\n"
    "v_mfma_f32_32x32x16_bf16 a[144:159], a[200:203], v[236:239], a[144:159]\n"
    "v_cvt_pk_bf16_f32 v228, v104, v105\n"
    "v_cvt_pk_bf16_f32 v229, v106, v107\n"
    "v_cvt_pk_bf16_f32 v230, v108, v109\n"
    "v_cvt_pk_bf16_f32 v231, v110, v111\n"
    "v_add_f32 v180, v180, v172\n"
    "v_mfma_f32_32x32x16_bf16 a[160:175], a[204:207], v[236:239], a[160:175]\n"
    "ds_read_b128 a[196:199], v192 offset:4096\n"
    "v_add_f32 v181, v181, v173\n"
    "v_lshlrev_b32 v172, 16, v164\n"
    "v_and_b32 v173, s46, v164\n"
    "v_add_f32 v182, v182, v172\n"
    "v_mfma_f32_32x32x16_bf16 v[112:127], v[80:83], v[48:51], 0\n"
    "v_add_f32 v183, v183, v173\n"
    "ds_read_b128 a[204:207], v192 offset:6144\n"
    "v_lshlrev_b32 v172, 16, v165\n"
    "v_and_b32 v173, s46, v165\n"
    "v_add_f32 v184, v184, v172\n"
    "v_mfma_f32_32x32x16_bf16 v[112:127], v[84:87], v[32:35], v[112:127]\n"
    "v_add_f32 v185, v185, v173\n"
    "v_lshlrev_b32 v172, 16, v166\n"
    "ds_read_b128 a[192:195], v29 offset:4096\n"
    "v_and_b32 v173, s46, v166\n"
    "v_add_f32 v186, v186, v172\n"
    "v_mfma_f32_32x32x16_bf16 v[112:127], v[88:91], v[52:55], v[112:127]\n"
    "v_add_f32 v187, v187, v173\n"
    "v_lshlrev_b32 v172, 16, v168\n"
    "v_and_b32 v173, s46, v168\n"
    "s_waitcnt lgkmcnt(13)\n"
    "ds_read_b128 a[200:203], v29 offset:6144\n"
    "v_add_f32 v188, v188, v172\n"
    "v_mfma_f32_32x32x16_bf16 v[112:127], v[92:95], v[36:39], v[112:127]\n"
    "v_add_f32 v189, v189, v173\n"
    "v_lshlrev_b32 v172, 16, v169\n"
    "v_and_b32 v173, s46, v169\n"
    "v_add_f32 v190, v190, v172\n"
    "v_add_f32 v191, v191, v173\n"
    "s_waitcnt lgkmcnt(12)\n"
    "s_waitcnt lgkmcnt(4)\n"
    "v_mfma_f32_32x32x16_bf16 a[144:159], a[176:179], v[224:227], a[144:159]\n"
    "v_add_f32 v176, v176, v208\n"
    "v_add_f32 v177, v177, v209\n"
    "v_add_f32 v178, v178, v210\n"
    "v_add_f32 v179, v179, v211\n"
    "v_add_f32 v180, v180, v212\n"
    "v_add_f32 v181, v181, v213\n"
    "v_mfma_f32_32x32x16_bf16 a[160:175], a[180:183], v[224:227], a[160:175]\n"
    "s_nop 0\n"
    "v_cvt_pk_bf16_f32 v232, v112, v113\n"
    "v_cvt_pk_bf16_f32 v233, v114, v115\n"
    "v_cvt_pk_bf16_f32 v234, v116, v117\n"
    "v_cvt_pk_bf16_f32 v235, v118, v119\n"
    "v_add_f32 v182, v182, v214\n"
    "v_mfma_f32_32x32x16_bf16 a[144:159], a[184:187], v[228:231], a[144:159]\n"
    "v_cvt_pk_bf16_f32 v236, v120, v121\n"
    "v_cvt_pk_bf16_f32 v237, v122, v123\n"
    "v_cvt_pk_bf16_f32 v238, v124, v125\n"
    "v_cvt_pk_bf16_f32 v239, v126, v127\n"
    "v_add_f32 v183, v183, v215\n"
    "v_mfma_f32_32x32x16_bf16 a[160:175], a[188:191], v[228:231], a[160:175]\n"
    "v_add_f32 v184, v184, v216\n"
    "v_add_f32 v185, v185, v217\n"
    "v_add_f32 v186, v186, v218\n"
    "v_add_f32 v187, v187, v219\n"
    "v_add_f32 v188, v188, v220\n"
    "s_waitcnt lgkmcnt(0)\n"
    "v_mfma_f32_32x32x16_bf16 a[160:175], a[196:199], v[232:235], a[160:175]\n"
    "v_add_f32 v189, v189, v221\n"
    "v_add_f32 v190, v190, v222\n"
    "v_add_f32 v191, v191, v223\n"
    "s_mov_b32 s61, 0xbfb8aa3b\n"
    "s_mov_b32 s60, 0x48e00000\n"
    "v_mul_f32 v172, s61, v176\n"
    "v_mfma_f32_32x32x16_bf16 a[160:175], a[204:207], v[236:239], a[160:175]\n"
    "v_mul_f32 v173, s61, v177\n"
    "v_mul_f32 v174, s61, v178\n"
    "v_mul_f32 v175, s61, v179\n"
    "v_exp_f32 v172, v172\n"
    "v_mfma_f32_32x32x16_bf16 a[144:159], a[192:195], v[232:235], a[144:159]\n"
    "v_exp_f32 v173, v173\n"
    "v_exp_f32 v174, v174\n"
    "v_exp_f32 v175, v175\n"
    "v_mfma_f32_32x32x16_bf16 a[144:159], a[200:203], v[236:239], a[144:159]\n"
    "s_nop 4\n"
    "ds_write_b128 v2, a[160:163] offset:0\n"
    "v_lshlrev_b32 v208, 16, v196\n"
    "v_and_b32 v209, s46, v196\n"
    "v_lshlrev_b32 v210, 16, v197\n"
    "v_and_b32 v211, s46, v197\n"
    "v_fma_f32 v172, v172, s60, s60\n"
    "v_fma_f32 v173, v173, s60, s60\n"
    "v_fma_f32 v174, v174, s60, s60\n"
    "v_fma_f32 v175, v175, s60, s60\n"
    "v_rcp_f32 v172, v172\n"
    "ds_write_b128 v2, a[164:167] offset:1024\n"
    "v_rcp_f32 v173, v173\n"
    "v_rcp_f32 v174, v174\n"
    "v_rcp_f32 v175, v175\n"
    "v_mul_f32 v176, v176, v172\n"
    "v_mul_f32 v177, v177, v173\n"
    "v_mul_f32 v178, v178, v174\n"
    "v_mul_f32 v179, v179, v175\n"
    "v_mul_f32 v172, s61, v180\n"
    "ds_write_b128 v2, a[168:171] offset:2048\n"
    "v_mul_f32 v173, s61, v181\n"
    "v_mul_f32 v174, s61, v182\n"
    "v_mul_f32 v175, s61, v183\n"
    "v_exp_f32 v172, v172\n"
    "v_exp_f32 v173, v173\n"
    "v_exp_f32 v174, v174\n"
    "v_exp_f32 v175, v175\n"
    "ds_write_b128 v2, a[172:175] offset:3072\n"
    "v_lshlrev_b32 v212, 16, v198\n"
    "v_and_b32 v213, s46, v198\n"
    "v_lshlrev_b32 v214, 16, v199\n"
    "v_and_b32 v215, s46, v199\n"
    "v_fma_f32 v172, v172, s60, s60\n"
    "v_fma_f32 v173, v173, s60, s60\n"
    "v_fma_f32 v174, v174, s60, s60\n"
    "v_fma_f32 v175, v175, s60, s60\n"
    "v_rcp_f32 v172, v172\n"
    "v_rcp_f32 v173, v173\n"
    "v_rcp_f32 v174, v174\n"
    "v_rcp_f32 v175, v175\n"
    "v_mul_f32 v180, v180, v172\n"
    "v_mul_f32 v181, v181, v173\n"
    "v_mul_f32 v182, v182, v174\n"
    "v_mul_f32 v183, v183, v175\n"
    "v_mul_f32 v172, s61, v184\n"
    "v_mul_f32 v173, s61, v185\n"
    "v_mul_f32 v174, s61, v186\n"
    "v_mul_f32 v175, s61, v187\n"
    "v_exp_f32 v172, v172\n"
    "v_exp_f32 v173, v173\n"
    "v_exp_f32 v174, v174\n"
    "v_exp_f32 v175, v175\n"
    "v_accvgpr_read_b32 v96, a144\n"
    "v_accvgpr_read_b32 v97, a145\n"
    "v_accvgpr_read_b32 v98, a146\n"
    "v_accvgpr_read_b32 v99, a147\n"
    "v_accvgpr_read_b32 v100, a148\n"
    "v_accvgpr_read_b32 v101, a149\n"
    "v_accvgpr_read_b32 v102, a150\n"
    "v_accvgpr_read_b32 v103, a151\n"
    "v_accvgpr_read_b32 v104, a152\n"
    "v_accvgpr_read_b32 v105, a153\n"
    "v_accvgpr_read_b32 v106, a154\n"
    "v_accvgpr_read_b32 v107, a155\n"
    "v_accvgpr_read_b32 v108, a156\n"
    "v_accvgpr_read_b32 v109, a157\n"
    "v_accvgpr_read_b32 v110, a158\n"
    "v_accvgpr_read_b32 v111, a159\n"
    "s_waitcnt lgkmcnt(0)\n"
    "s_barrier\n"
    "ds_read_b128 v[112:115], v3 offset:0\n"
    "ds_read_b128 v[116:119], v3 offset:1024\n"
    "ds_read_b128 v[120:123], v3 offset:2048\n"
    "ds_read_b128 v[124:127], v3 offset:3072\n"
    "v_lshlrev_b32 v216, 16, v200\n"
    "v_and_b32 v217, s46, v200\n"
    "v_lshlrev_b32 v218, 16, v201\n"
    "v_and_b32 v219, s46, v201\n"
    "v_fma_f32 v172, v172, s60, s60\n"
    "v_fma_f32 v173, v173, s60, s60\n"
    "v_fma_f32 v174, v174, s60, s60\n"
    "v_fma_f32 v175, v175, s60, s60\n"
    "v_rcp_f32 v172, v172\n"
    "v_rcp_f32 v173, v173\n"
    "v_rcp_f32 v174, v174\n"
    "v_rcp_f32 v175, v175\n"
    "v_mul_f32 v184, v184, v172\n"
    "v_mul_f32 v185, v185, v173\n"
    "v_mul_f32 v186, v186, v174\n"
    "v_mul_f32 v187, v187, v175\n"
    "v_mul_f32 v172, s61, v188\n"
    "v_mul_f32 v173, s61, v189\n"
    "v_mul_f32 v174, s61, v190\n"
    "v_mul_f32 v175, s61, v191\n"
    "v_exp_f32 v172, v172\n"
    "v_exp_f32 v173, v173\n"
    "v_exp_f32 v174, v174\n"
    "v_exp_f32 v175, v175\n"
    "v_lshlrev_b32 v220, 16, v202\n"
    "v_and_b32 v221, s46, v202\n"
    "v_lshlrev_b32 v222, 16, v203\n"
    "v_and_b32 v223, s46, v203\n"
    "v_fma_f32 v172, v172, s60, s60\n"
    "v_fma_f32 v173, v173, s60, s60\n"
    "v_fma_f32 v174, v174, s60, s60\n"
    "v_fma_f32 v175, v175, s60, s60\n"
    "v_rcp_f32 v172, v172\n"
    "v_rcp_f32 v173, v173\n"
    "v_rcp_f32 v174, v174\n"
    "v_rcp_f32 v175, v175\n"
    "v_mul_f32 v188, v188, v172\n"
    "v_mul_f32 v189, v189, v173\n"
    "v_mul_f32 v190, v190, v174\n"
    "v_mul_f32 v191, v191, v175\n"
    "v_pk_fma_f32 v[208:209], v[176:177], v[96:97], v[208:209]\n"
    "v_pk_fma_f32 v[210:211], v[178:179], v[98:99], v[210:211]\n"
    "v_pk_fma_f32 v[212:213], v[180:181], v[100:101], v[212:213]\n"
    "v_pk_fma_f32 v[214:215], v[182:183], v[102:103], v[214:215]\n"
    "v_pk_fma_f32 v[216:217], v[184:185], v[104:105], v[216:217]\n"
    "v_pk_fma_f32 v[218:219], v[186:187], v[106:107], v[218:219]\n"
    "v_pk_fma_f32 v[220:221], v[188:189], v[108:109], v[220:221]\n"
    "v_pk_fma_f32 v[222:223], v[190:191], v[110:111], v[222:223]\n"
    "s_waitcnt lgkmcnt(2)\n"
    "v_pk_fma_f32 v[208:209], v[176:177], v[112:113], v[208:209]\n"
    "v_pk_fma_f32 v[210:211], v[178:179], v[114:115], v[210:211]\n"
    "v_pk_fma_f32 v[212:213], v[180:181], v[116:117], v[212:213]\n"
    "v_pk_fma_f32 v[214:215], v[182:183], v[118:119], v[214:215]\n"
    "v_cvt_pk_bf16_f32 v80, v208, v209\n"
    "v_cvt_pk_bf16_f32 v81, v210, v211\n"
    "v_cvt_pk_bf16_f32 v82, v212, v213\n"
    "v_cvt_pk_bf16_f32 v83, v214, v215\n"
    "s_nop 0\n"
    "v_permlane32_swap_b32 v80, v82\n"
    "v_permlane32_swap_b32 v81, v83\n"
    "buffer_store_dwordx4 v[80:83], v1, s[52:55], 0 offen offset:0\n"
    "s_waitcnt lgkmcnt(0)\n"
    "v_pk_fma_f32 v[216:217], v[184:185], v[120:121], v[216:217]\n"
    "v_pk_fma_f32 v[218:219], v[186:187], v[122:123], v[218:219]\n"
    "v_pk_fma_f32 v[220:221], v[188:189], v[124:125], v[220:221]\n"
    "v_pk_fma_f32 v[222:223], v[190:191], v[126:127], v[222:223]\n"
    "v_cvt_pk_bf16_f32 v84, v216, v217\n"
    "v_cvt_pk_bf16_f32 v85, v218, v219\n"
    "v_cvt_pk_bf16_f32 v86, v220, v221\n"
    "v_cvt_pk_bf16_f32 v87, v222, v223\n"
    "s_nop 0\n"
    "v_permlane32_swap_b32 v84, v86\n"
    "v_permlane32_swap_b32 v85, v87\n"
    "buffer_store_dwordx4 v[84:87], v1, s[52:55], 0 offen offset:32\n"
    "v_cmp_eq_u32 vcc, 0, v16\n"
    "v_mov_b32 v22, 0x7ffffff0\n"
    "s_nop 0\n"
    "v_cndmask_b32 v22, v22, 0, vcc\n"
    "buffer_store_dwordx2 v[46:47], v22, s[48:51], 0 offen offset:0 sc1\n"
    "s_branch .Lm_end\n"
    ".Lm_st_slow:\n"
    "s_mov_b32 s66, 32768\n"
    ".Lm_st_loop:\n"
    "buffer_load_dwordx4 v[64:67], v45, s[48:51], 0 offen offset:0 sc1\n"
    "buffer_load_dwordx4 v[68:71], v198, s[48:51], 0 offen offset:0 sc1\n"
    "s_waitcnt vmcnt(0)\n"
    "v_cmp_ne_u32 vcc, s62, v66\n"
    "v_cmp_ne_u32 s[64:65], s63, v67\n"
    "s_nop 3\n"
    "s_or_b64 vcc, vcc, s[64:65]\n"
    "v_cmp_ne_u32 s[64:65], s62, v70\n"
    "s_nop 3\n"
    "s_or_b64 vcc, vcc, s[64:65]\n"
    "v_cmp_ne_u32 s[64:65], s63, v71\n"
    "s_nop 3\n"
    "s_or_b64 vcc, vcc, s[64:65]\n"
    "s_cbranch_vccz .Lm_st_ok\n"
    "s_sleep 1\n"
    "s_sub_u32 s66, s66, 1\n"
    "s_cmp_lg_u32 s66, 0\n"
    "s_cbranch_scc1 .Lm_st_loop\n"
    "s_branch .Lm_st_ok\n"
    ".Lm_cs_slow:\n"
    "s_mov_b32 s66, 32768\n"
    ".Lm_cs_loop:\n"
    "buffer_load_dwordx4 v[252:255], v65, s[48:51], 0 offen offset:0 sc1\n"
    "buffer_load_dwordx4 v[92:95], v65, s[48:51], 0 offen offset:1024 sc1\n"
    "s_waitcnt vmcnt(0)\n"
    "v_cmp_ne_u32 vcc, s62, v253\n"
    "v_cmp_ne_u32 s[64:65], s63, v255\n"
    "s_nop 3\n"
    "s_or_b64 vcc, vcc, s[64:65]\n"
    "v_cmp_ne_u32 s[64:65], s62, v93\n"
    "s_nop 3\n"
    "s_or_b64 vcc, vcc, s[64:65]\n"
    "v_cmp_ne_u32 s[64:65], s63, v95\n"
    "s_nop 3\n"
    "s_or_b64 vcc, vcc, s[64:65]\n"
    "s_cbranch_vccz .Lm_cs_ok\n"
    "s_sleep 1\n"
    "s_sub_u32 s66, s66, 1\n"
    "s_cmp_lg_u32 s66, 0\n"
    "s_cbranch_scc1 .Lm_cs_loop\n"
    "s_branch .Lm_cs_ok\n"
    ".Lm_h0_slow:\n"
    "s_mov_b32 s66, 32768\n"
    ".Lm_h0_loop:\n"
    "buffer_load_dwordx2 v[22:23], v14, s[48:51], 0 offen offset:0 sc1\n"
    "s_waitcnt vmcnt(0)\n"
    "v_cmp_ne_u32 vcc, s62, v22\n"
    "v_cmp_ne_u32 s[64:65], s63, v23\n"
    "s_nop 3\n"
    "s_or_b64 vcc, vcc, s[64:65]\n"
    "s_cbranch_vccz .Lm_h0_ok\n"
    "s_sleep 1\n"
    "s_sub_u32 s66, s66, 1\n"
    "s_cmp_lg_u32 s66, 0\n"
    "s_cbranch_scc1 .Lm_h0_loop\n"
    "s_branch .Lm_h0_ok\n"
    ".Lm_h1_slow:\n"
    "s_mov_b32 s66, 32768\n"
    ".Lm_h1_loop:\n"
    "buffer_load_dwordx2 v[22:23], v14, s[48:51], 0 offen offset:64 sc1\n"
    "s_waitcnt vmcnt(0)\n"
    "v_cmp_ne_u32 vcc, s62, v22\n"
    "v_cmp_ne_u32 s[64:65], s63, v23\n"
    "s_nop 3\n"
    "s_or_b64 vcc, vcc, s[64:65]\n"
    "s_cbranch_vccz .Lm_h1_ok\n"
    "s_sleep 1\n"
    "s_sub_u32 s66, s66, 1\n"
    "s_cmp_lg_u32 s66, 0\n"
    "s_cbranch_scc1 .Lm_h1_loop\n"
    "s_branch .Lm_h1_ok\n"
    ".Lm_u_slow:\n"
    "s_mov_b32 s66, 32768\n"
    ".Lm_u_loop:\n"
    "buffer_load_dwordx2 v[204:205], v15, s[48:51], 0 offen offset:0 sc1\n"
    "s_waitcnt vmcnt(0)\n"
    "v_cmp_ne_u32 vcc, s62, v204\n"
    "v_cmp_ne_u32 s[64:65], s63, v205\n"
    "s_nop 3\n"
    "s_or_b64 vcc, vcc, s[64:65]\n"
    "s_cbranch_vccz .Lm_u_ok\n"
    "s_sleep 1\n"
    "s_sub_u32 s66, s66, 1\n"
    "s_cmp_lg_u32 s66, 0\n"
    "s_cbranch_scc1 .Lm_u_loop\n"
    "s_branch .Lm_u_ok\n"
    ".Lm_x_slow:\n"
    "s_mov_b32 s66, 32768\n"
    ".Lm_x_loop:\n"
    "buffer_load_dwordx4 v[160:163], v4, s[48:51], 0 offen offset:0 sc1\n"
    "buffer_load_dwordx4 v[164:167], v4, s[48:51], 0 offen offset:1024 sc1\n"
    "buffer_load_dwordx4 v[168:171], v4, s[48:51], 0 offen offset:2048 sc1\n"
    "s_waitcnt vmcnt(0)\n"
    "v_cmp_ne_u32 vcc, s62, v163\n"
    "v_cmp_ne_u32 s[64:65], s62, v167\n"
    "s_nop 3\n"
    "s_or_b64 vcc, vcc, s[64:65]\n"
    "v_cmp_ne_u32 s[64:65], s62, v171\n"
    "s_nop 3\n"
    "s_or_b64 vcc, vcc, s[64:65]\n"
    "s_cbranch_vccz .Lm_x_ok\n"
    "s_sleep 1\n"
    "s_sub_u32 s66, s66, 1\n"
    "s_cmp_lg_u32 s66, 0\n"
    "s_cbranch_scc1 .Lm_x_loop\n"
    "s_branch .Lm_x_ok\n"
    ".Lm_end:\n"
    :: "s"(ka), "s"(wsel), "s"(0), "s"(0), "s"(0), "s"(w), "s"(rt), "s"(ct), "s"(kh), "s"(cb), "s"(blk), "s"(b),
       "s"((unsigned)(size_t)smem)
    : "v0", "v1", "v2", "v3", "v4", "v5", "v6", "v7", "v8", "v9", "v10", "v11", "v12", "v13", "v14", "v15", "v16", "v17", "v18", "v19", "v20", "v21", "v22", "v23", "v24", "v25", "v26", "v27", "v28", "v29", "v30", "v31", "v32", "v33", "v34", "v35", "v36", "v37", "v38", "v39", "v40", "v41", "v42", "v43", "v44", "v45", "v46", "v47", "v48", "v49", "v50", "v51", "v52", "v53", "v54", "v55", "v56", "v57", "v58", "v59", "v60", "v61", "v62", "v63", "v64", "v65", "v66", "v67", "v68", "v69", "v70", "v71", "v72", "v73", "v74", "v75", "v76", "v77", "v78", "v79", "v80", "v81", "v82", "v83", "v84", "v85", "v86", "v87", "v88", "v89", "v90", "v91", "v92", "v93", "v94", "v95", "v96", "v97", "v98", "v99", "v100", "v101", "v102", "v103", "v104", "v105", "v106", "v107", "v108", "v109", "v110", "v111", "v112", "v113", "v114", "v115", "v116", "v117", "v118", "v119", "v120", "v121", "v122", "v123", "v124", "v125", "v126", "v127", "v128", "v129", "v130", "v131", "v132", "v133", "v134", "v135", "v136", "v137", "v138", "v139", "v140", "v141", "v142", "v143", "v144", "v145", "v146", "v147", "v148", "v149", "v150", "v151", "v152", "v153", "v154", "v155", "v156", "v157", "v158", "v159", "v160", "v161", "v162", "v163", "v164", "v165", "v166", "v167", "v168", "v169", "v170", "v171", "v172", "v173", "v174", "v175", "v176", "v177", "v178", "v179", "v180", "v181", "v182", "v183", "v184", "v185", "v186", "v187", "v188", "v189", "v190", "v191", "v192", "v193", "v194", "v195", "v196", "v197", "v198", "v199", "v200", "v201", "v202", "v203", "v204", "v205", "v206", "v207", "v208", "v209", "v210", "v211", "v212", "v213", "v214", "v215", "v216", "v217", "v218", "v219", "v220", "v221", "v222", "v223", "v224", "v225", "v226", "v227", "v228", "v229", "v230", "v231", "v232", "v233", "v234", "v235", "v236", "v237", "v238", "v239", "v240", "v241", "v242", "v243", "v244", "v245", "v246", "v247", "v248", "v249", "v250", "v251", "v252", "v253", "v254", "v255", "a0", "a1", "a2", "a3", "a4", "a5", "a6", "a7", "a8", "a9", "a10", "a11", "a12", "a13", "a14", "a15", "a16", "a17", "a18", "a19", "a20", "a21", "a22", "a23", "a24", "a25", "a26", "a27", "a28", "a29", "a30", "a31", "a32", "a33", "a34", "a35", "a36", "a37", "a38", "a39", "a40", "a41", "a42", "a43", "a44", "a45", "a46", "a47", "a48", "a49", "a50", "a51", "a52", "a53", "a54", "a55", "a56", "a57", "a58", "a59", "a60", "a61", "a62", "a63", "a64", "a65", "a66", "a67", "a68", "a69", "a70", "a71", "a72", "a73", "a74", "a75", "a76", "a77", "a78", "a79", "a80", "a81", "a82", "a83", "a84", "a85", "a86", "a87", "a88", "a89", "a90", "a91", "a92", "a93", "a94", "a95", "a96", "a97", "a98", "a99", "a100", "a101", "a102", "a103", "a104", "a105", "a106", "a107", "a108", "a109", "a110", "a111", "a112", "a113", "a114", "a115", "a116", "a117", "a118", "a119", "a120", "a121", "a122", "a123", "a124", "a125", "a126", "a127", "a128", "a129", "a130", "a131", "a132", "a133", "a134", "a135", "a136", "a137", "a138", "a139", "a140", "a141", "a142", "a143", "a144", "a145", "a146", "a147", "a148", "a149", "a150", "a151", "a152", "a153", "a154", "a155", "a156", "a157", "a158", "a159", "a160", "a161", "a162", "a163", "a164", "a165", "a166", "a167", "a168", "a169", "a170", "a171", "a172", "a173", "a174", "a175", "a176", "a177", "a178", "a179", "a180", "a181", "a182", "a183", "a184", "a185", "a186", "a187", "a188", "a189", "a190", "a191", "a192", "a193", "a194", "a195", "a196", "a197", "a198", "a199", "a200", "a201", "a202", "a203", "a204", "a205", "a206", "a207", "a208", "a209", "a210", "a211", "a212", "a213", "a214", "a215", "a216", "a217", "a218", "a219", "a220", "a221", "a222", "a223", "a224", "a225", "a226", "a227", "a228", "a229", "a230", "a231", "a232", "a233", "a234", "a235", "a236", "a237", "a238", "a239", "a240", "a241", "a242", "a243", "a244", "a245", "a246", "a247", "a248", "a249", "a250", "a251", "a252", "a253", "a254", "a255", "s30", "s31", "s33", "s34", "s35", "s36", "s37", "s38", "s39", "s40", "s41", "s42", "s43", "s44", "s45", "s46", "s47", "s48", "s49", "s50", "s51", "s52", "s53", "s54", "s55", "s56", "s57", "s58", "s59", "s60", "s61", "s62", "s63", "s64", "s65", "s66", "s67", "s68", "s69", "s70", "s71", "s72", "s73", "s74", "s75", "s76", "s77", "s78", "s79", "s80", "s81", "s82", "s83", "s84", "s85", "s86", "s87", "s88", "s89", "s90", "s91", "s92", "s93", "s94", "s95", "s96", "s97", "s98", "s99", "vcc", "scc", "memory");
}

extern "C" void kernel_launch(void* const* d_in, const int* in_sizes, int n_in, void* d_out, int out_size, void* d_ws,
                              size_t ws_size, hipStream_t stream) {
  fused_kernel<<<256, 256, 0, stream>>>((const char*)d_in[0], (const u16*)d_in[1], (const u16*)d_in[2],
                                        (const char*)d_in[4], (const char*)d_in[5], (const char*)d_in[6],
                                        (char*)d_out, (char*)d_ws);
}
```
